```python
import torch

def _softmax(x, axis=-1):
    return torch.softmax(x, dim=axis)
def _gelu(x):
    return torch.nn.functional.gelu(x, approximate="tanh")
def _silu(x):
    return torch.nn.functional.silu(x)
def _sigmoid(x):
    return torch.sigmoid(x)
def _layernorm(x, axis=-1, eps=1e-5):
    m = x.mean(dim=axis, keepdim=True)
    v = x.var(dim=axis, keepdim=True, unbiased=False)
    return (x - m) / torch.sqrt(v + eps)
def _scatter_add(tgt, idx, src, axis=0):
    return tgt.clone().index_add_(axis, idx, src)
def _segment_sum(src, seg, n):
    out = torch.zeros((n,) + src.shape[1:], dtype=src.dtype)
    return out.index_add_(0, seg, src)
def _pad(x, pw):
    flat = tuple(v for pair in reversed(pw) for v in pair)
    return torch.nn.functional.pad(x, flat)

kernel_name = 'recurrent_scan__sliding_block_cached__dense_activation_gelu__decode'

def setup_inputs():

    _w = (lambda a: (lambda t: t.to(torch.bfloat16) if t.is_floating_point() else t)(torch.as_tensor(a)))
    def _A_setup():
        B, Sq, D = 8, 1, 1024
        Hq, Hkv, Sk, Dh = 32, 4, 4096, 128
        assert Hq % Hkv == 0, (Hq, Hkv)
        return {
            "x": _w((lambda *s: torch.randn(*s, dtype=torch.bfloat16) * 0.05)(B, Sq, D)),
            "k_cache": _w((lambda *s: torch.randn(*s, dtype=torch.bfloat16) * 3.2)(B, Hkv, Sk, Dh)),
            "v_cache": _w((lambda *s: torch.randn(*s, dtype=torch.bfloat16) * 3.2)(B, Hkv, Sk, Dh)),
            "wq": _w((lambda *s: torch.randn(*s, dtype=torch.bfloat16) * 3.2)(D, Hq * Dh) * (D ** -0.5)),
            "wo": _w((lambda *s: torch.randn(*s, dtype=torch.bfloat16) * 3.2)(Hq * Dh, D) * ((Hq * Dh) ** -0.5)),
        }
    _A_in = _A_setup()

    def _B_setup():
        B, S, D, F = 8, 1, 1024, 3584
        return {
            "x": _w((lambda *s: torch.randn(*s, dtype=torch.bfloat16) * 0.05)(B, S, D)),
            "w1": _w((lambda *s: torch.randn(*s, dtype=torch.bfloat16) * 3.2)(D, F) * (D ** -0.5)),
            "b1": _w((lambda *s: torch.randn(*s, dtype=torch.bfloat16) * 3.2)(F) * 0.01),
            "w2": _w((lambda *s: torch.randn(*s, dtype=torch.bfloat16) * 0.8)(F, D) * (F ** -0.5)),
            "b2": _w((lambda *s: torch.randn(*s, dtype=torch.bfloat16) * 0.8)(D) * 0.01),
        }
    _B_in = _B_setup()

    return {
        "A_x": _A_in["x"],
        "A_k_cache": _A_in["k_cache"],
        "A_v_cache": _A_in["v_cache"],
        "A_wq": _A_in["wq"],
        "A_wo": _A_in["wo"],
        "B_x": _B_in["x"],
        "B_w1": _B_in["w1"],
        "B_b1": _B_in["b1"],
        "B_w2": _B_in["w2"],
        "B_b2": _B_in["b2"],
    }

def reference(A_x, A_k_cache, A_v_cache, A_wq, A_wo, B_x, B_w1, B_b1, B_w2, B_b2):
    def _A_ref(x, k_cache, v_cache, wq, wo):
        B, Sq, D = x.shape
        Hq, Dh = 32, 128
        Hkv, Sk = k_cache.shape[1], k_cache.shape[2]
        W = min(4096, max(256, Sk // 4))
        rep = Hq // Hkv
        q = (lambda x, *d: x.permute(*d))((x @ wq).reshape(B, Sq, Hq, Dh), 0, 2, 1, 3)
        k = (lambda x, n, axis: torch.repeat_interleave(x, n, dim=axis))(k_cache[:, :, Sk - W:, :], rep, axis=1)
        v = (lambda x, n, axis: torch.repeat_interleave(x, n, dim=axis))(v_cache[:, :, Sk - W:, :], rep, axis=1)
        scores = torch.einsum("bhqd,bhkd->bhqk", q, k) * (Dh ** -0.5)
        p = _softmax(scores, axis=-1)
        o = torch.einsum("bhqk,bhkd->bhqd", p, v)
        return (lambda x, *d: x.permute(*d))(o, 0, 2, 1, 3).reshape(B, Sq, Hq * Dh) @ wo

    def _B_ref(x, w1, b1, w2, b2):
        h = torch.einsum("bsd,df->bsf", x, w1) + b1
        h = _gelu(h)
        return torch.einsum("bsf,fd->bsd", h, w2) + b2

    x = A_x
    xn = _layernorm(x, axis=-1)
    s = _A_ref(xn, A_k_cache, A_v_cache, A_wq, A_wo)
    g = _silu(_B_ref(xn, B_w1, B_b1, B_w2, B_b2))
    return x + s * g
```

```cpp
#include <hip/hip_runtime.h>
#include <hip/hip_bf16.h>
#include <stdint.h>

typedef unsigned short u16;
__device__ __forceinline__ float bf2f(u16 b) { return __uint_as_float(((unsigned)b) << 16); }
__device__ __forceinline__ float rbf(float f) {
  return __bfloat162float(__float2bfloat16(f));
}
#define MIMIC 0
__device__ __forceinline__ float R(float f) { return MIMIC ? rbf(f) : f; }

constexpr int B = 8, D = 1024, HQ = 32, HKV = 4, SK = 4096, DH = 128, W = 1024, F = 3584;

__global__ void k_ln(const u16* x, float* xn) {
  int b = blockIdx.x; int t = threadIdx.x;
  __shared__ float red[256];
  float v[4]; float s = 0;
  for (int i = 0; i < 4; i++) { v[i] = bf2f(x[b * D + t * 4 + i]); s += v[i]; }
  red[t] = s; __syncthreads();
  for (int o = 128; o > 0; o >>= 1) { if (t < o) red[t] += red[t + o]; __syncthreads(); }
  float mean = red[0] / D; __syncthreads();
  float ss = 0; for (int i = 0; i < 4; i++) { float d = v[i] - mean; ss += d * d; }
  red[t] = ss; __syncthreads();
  for (int o = 128; o > 0; o >>= 1) { if (t < o) red[t] += red[t + o]; __syncthreads(); }
  float var = red[0] / D;
  float m = R(mean), vv = R(var);
  float den = R(sqrtf(R(vv + 1e-5f)));
  for (int i = 0; i < 4; i++) xn[b * D + t * 4 + i] = rbf(R(v[i] - m) / den);
}
__global__ void k_gemv(const float* a, const u16* w, float* y, int K, int N) {
  int n = blockIdx.x * 64 + (threadIdx.x & 63); int ks = threadIdx.x >> 6;
  __shared__ float red[4][8][64];
  float acc[8] = {0};
  for (int k = ks; k < K; k += 4) {
    float wv = bf2f(w[(size_t)k * N + n]);
    for (int b = 0; b < 8; b++) acc[b] += a[b * K + k] * wv;
  }
  for (int b = 0; b < 8; b++) red[ks][b][threadIdx.x & 63] = acc[b];
  __syncthreads();
  if (ks == 0) for (int b = 0; b < 8; b++) { int l = threadIdx.x; y[b * N + n] = red[0][b][l] + red[1][b][l] + red[2][b][l] + red[3][b][l]; }
}
__global__ void k_attn(const float* qf, const u16* kc, const u16* vc, float* o) {
  int b = blockIdx.x / HQ, h = blockIdx.x % HQ, kvh = h / 8; int t = threadIdx.x;
  __shared__ float q[DH]; __shared__ float sc[W]; __shared__ float red[256];
  if (t < DH) q[t] = R(qf[b * 4096 + h * DH + t]);
  __syncthreads();
  const u16* kb = kc + ((size_t)(b * HKV + kvh) * SK + (SK - W)) * DH;
  const u16* vb = vc + ((size_t)(b * HKV + kvh) * SK + (SK - W)) * DH;
  float mx = -INFINITY;
  for (int j = t; j < W; j += 256) {
    float s = 0; for (int d = 0; d < DH; d++) s += q[d] * bf2f(kb[(size_t)j * DH + d]);
    s = R(R(s) * 0.08838834764831845f); sc[j] = s; mx = fmaxf(mx, s);
  }
  red[t] = mx; __syncthreads();
  for (int of = 128; of > 0; of >>= 1) { if (t < of) red[t] = fmaxf(red[t], red[t + of]); __syncthreads(); }
  mx = red[0]; __syncthreads();
  float sum = 0;
  for (int j = t; j < W; j += 256) { float e = expf(sc[j] - mx); sc[j] = e; sum += e; }
  red[t] = sum; __syncthreads();
  for (int of = 128; of > 0; of >>= 1) { if (t < of) red[t] += red[t + of]; __syncthreads(); }
  sum = red[0]; __syncthreads();
  for (int j = t; j < W; j += 256) sc[j] = R(sc[j] / sum);
  __syncthreads();
  if (t < DH) { float a = 0; for (int j = 0; j < W; j++) a += sc[j] * bf2f(vb[(size_t)j * DH + t]); o[b * 4096 + h * DH + t] = R(a); }
}
__global__ void k_gelu(float* h, const u16* b1) {
  int i = blockIdx.x * 256 + threadIdx.x; int f = i % F;
  float v = R(R(h[i]) + bf2f(b1[f]));
  float c = 0.7978845608028654f * (v + 0.044715f * v * v * v);
  h[i] = R(0.5f * v * (1.f + tanhf(c)));
}
__global__ void k_final(const u16* x, const float* s, const float* y, const u16* b2, __hip_bfloat16* out) {
  int i = blockIdx.x * 256 + threadIdx.x; int d = i % D;
  float yy = R(R(y[i]) + bf2f(b2[d]));
  float g = R(yy / (1.f + expf(-yy)));
  float r = R(R(s[i]) * g);
  out[i] = __float2bfloat16(bf2f(x[i]) + r);
}

extern "C" void kernel_launch(void* const* d_in, const int* in_sizes, int n_in,
                              void* d_out, int out_size, void* d_ws, size_t ws_size,
                              hipStream_t stream) {
  const u16* x = (const u16*)d_in[0]; const u16* kc = (const u16*)d_in[1]; const u16* vc = (const u16*)d_in[2];
  const u16* wq = (const u16*)d_in[3]; const u16* wo = (const u16*)d_in[4];
  const u16* w1 = (const u16*)d_in[6]; const u16* b1 = (const u16*)d_in[7]; const u16* w2 = (const u16*)d_in[8]; const u16* b2 = (const u16*)d_in[9];
  float* ws = (float*)d_ws;
  float* xn = ws; float* q = xn + 8192; float* o = q + 32768; float* s = o + 32768; float* h = s + 8192; float* y = h + 8 * F;
  k_ln<<<8, 256, 0, stream>>>(x, xn);
  k_gemv<<<4096 / 64, 256, 0, stream>>>(xn, wq, q, 1024, 4096);
  k_gemv<<<F / 64, 256, 0, stream>>>(xn, w1, h, 1024, F);
  k_attn<<<256, 256, 0, stream>>>(q, kc, vc, o);
  k_gelu<<<8 * F / 256, 256, 0, stream>>>(h, b1);
  k_gemv<<<1024 / 64, 256, 0, stream>>>(o, wo, s, 4096, 1024);
  k_gemv<<<1024 / 64, 256, 0, stream>>>(h, w2, y, F, 1024);
  k_final<<<32, 256, 0, stream>>>(x, s, y, b2, (__hip_bfloat16*)d_out);
}
```

```cpp
#include <hip/hip_runtime.h>
#include <hip/hip_bf16.h>
#include <stdint.h>

typedef unsigned v4u __attribute__((ext_vector_type(4)));
typedef unsigned v2u __attribute__((ext_vector_type(2)));
typedef float f32x4 __attribute__((ext_vector_type(4)));
typedef __bf16 bf16x8 __attribute__((ext_vector_type(8)));
typedef short s16x4 __attribute__((ext_vector_type(4)));
typedef unsigned short u16;
#define LAS __attribute__((address_space(3)))

#define W_AUX 0
#define SC1 16
#define SPIN_CAP 20000

constexpr unsigned WS_EPOCH = 0;
constexpr unsigned WS_QP = 4096;
constexpr unsigned WS_HP = WS_QP + 4 * 8 * 4096 * 4;
constexpr unsigned WS_OP = WS_HP + 4 * 8 * 3584 * 4;
constexpr unsigned WS_ML = WS_OP + 32 * 8 * 8 * 128 * 4;
constexpr unsigned WS_SP = WS_ML + 32 * 8 * 8 * 16;
constexpr unsigned WS_YP = WS_SP + 32 * 8 * 1024 * 4;
constexpr unsigned WS_END = WS_YP + 28 * 8 * 1024 * 4;
constexpr unsigned WS_STAMP = 8u << 20;

constexpr int L_XS0 = 0;
constexpr int L_XS = L_XS0 + 8 * 528;
constexpr int L_QS = L_XS + 8 * 272;
constexpr int L_MX = L_QS + 8 * 272;
constexpr int L_LS = L_MX + 256;
constexpr int L_FIN = L_LS + 256;
constexpr int L_RED = L_FIN + 1024;
constexpr int RED_BYTES = 8 * 8 * 132 * 4;
constexpr int L_BYTES = L_RED + 3 * RED_BYTES;

struct Params {
  const u16 *x, *kc, *vc, *wq, *wo, *w1, *b1, *w2, *b2;
  u16* out;
  unsigned char* ws;
};

__device__ __forceinline__ float bf_lo(unsigned w) { return __uint_as_float(w << 16); }
__device__ __forceinline__ float bf_hi(unsigned w) { return __uint_as_float(w & 0xffff0000u); }
__device__ __forceinline__ unsigned pk_bf16(float lo, float hi) {
  typedef __bf16 bf2 __attribute__((ext_vector_type(2)));
  typedef float f2 __attribute__((ext_vector_type(2)));
  f2 v = {lo, hi};
  return __builtin_bit_cast(unsigned, __builtin_convertvector(v, bf2));
}
__device__ __forceinline__ float rbf(float f) { return bf_lo(pk_bf16(f, f)); }
template <int CTRL> __device__ __forceinline__ float dpp(float x) {
  return __builtin_bit_cast(float, __builtin_amdgcn_mov_dpp(__builtin_bit_cast(int, x), CTRL, 0xf, 0xf, true));
}
__device__ __forceinline__ float x16_sum(float x) {
  auto s = __builtin_amdgcn_permlane16_swap(__float_as_uint(x), __float_as_uint(x), false, false);
  return __uint_as_float(s[0]) + __uint_as_float(s[1]);
}
__device__ __forceinline__ float x32_sum(float x) {
  auto s = __builtin_amdgcn_permlane32_swap(__float_as_uint(x), __float_as_uint(x), false, false);
  return __uint_as_float(s[0]) + __uint_as_float(s[1]);
}
__device__ __forceinline__ float x16_max(float x) {
  auto s = __builtin_amdgcn_permlane16_swap(__float_as_uint(x), __float_as_uint(x), false, false);
  return fmaxf(__uint_as_float(s[0]), __uint_as_float(s[1]));
}
__device__ __forceinline__ float x32_max(float x) {
  auto s = __builtin_amdgcn_permlane32_swap(__float_as_uint(x), __float_as_uint(x), false, false);
  return fmaxf(__uint_as_float(s[0]), __uint_as_float(s[1]));
}
__device__ __forceinline__ float wave_sum(float v) {
  v += dpp<0xB1>(v); v += dpp<0x4E>(v); v += dpp<0x141>(v); v += dpp<0x140>(v);
  v = x16_sum(v); return x32_sum(v);
}
__device__ __forceinline__ unsigned tagw(float v, unsigned tb) { return ((__float_as_uint(v) + 0x80u) & 0xFFFFFF00u) | tb; }
__device__ __forceinline__ float untag(unsigned w) { return __uint_as_float(w & 0xFFFFFF00u); }
__device__ __forceinline__ v4u tag4(f32x4 v, unsigned tag) {
  v4u o; o.x = tagw(v.x, tag & 0xff); o.y = tagw(v.y, (tag >> 8) & 0xff); o.z = tagw(v.z, (tag >> 16) & 0xff); o.w = tagw(v.w, tag >> 24); return o;
}
__device__ __forceinline__ unsigned bad4(v4u u, unsigned tag) {
  const unsigned lo = __builtin_amdgcn_perm(u.y, u.x, 0x0c0c0400u), hi = __builtin_amdgcn_perm(u.w, u.z, 0x04000c0cu);
  return (lo | hi) ^ tag;
}
__device__ __forceinline__ unsigned bad2(v2u u, unsigned tag16) { return __builtin_amdgcn_perm(u.y, u.x, 0x0c0c0400u) ^ tag16; }
__device__ __forceinline__ f32x4 mfma32(v4u a, v4u b, f32x4 c) {
  return __builtin_amdgcn_mfma_f32_16x16x32_bf16(__builtin_bit_cast(bf16x8, a), __builtin_bit_cast(bf16x8, b), c, 0, 0, 0);
}
__device__ __forceinline__ f32x4 mfma16(v2u a, v2u b, f32x4 c) {
  return __builtin_amdgcn_mfma_f32_16x16x16bf16_1k(__builtin_bit_cast(s16x4, a), __builtin_bit_cast(s16x4, b), c, 0, 0, 0);
}
#define RSRC(p, n) __builtin_amdgcn_make_buffer_rsrc((void*)(p), 0, (n), 0x00020000)
#define SB() __builtin_amdgcn_sched_barrier(0)
#define CB() asm volatile("" ::: "memory")

__device__ __forceinline__ void tile_mfma_store(const v4u (&t)[8], v4u bfrag, LAS float* red, int lane) {
  f32x4 acc[8];
#pragma unroll
  for (int c = 0; c < 8; ++c) {
    const unsigned sel = (c & 1) ? 0x07060302u : 0x05040100u;
    v4u a;
    a.x = __builtin_amdgcn_perm(t[1][c >> 1], t[0][c >> 1], sel);
    a.y = __builtin_amdgcn_perm(t[3][c >> 1], t[2][c >> 1], sel);
    a.z = __builtin_amdgcn_perm(t[5][c >> 1], t[4][c >> 1], sel);
    a.w = __builtin_amdgcn_perm(t[7][c >> 1], t[6][c >> 1], sel);
    acc[c] = mfma32(a, bfrag, f32x4{0.f, 0.f, 0.f, 0.f});
  }
  if ((lane & 15) < 8) {
    LAS float* p = red + (lane & 15) * 132 + (lane >> 4) * 32;
#pragma unroll
    for (int r = 0; r < 4; ++r) {
      *(LAS f32x4*)(p + 8 * r) = f32x4{acc[0][r], acc[1][r], acc[2][r], acc[3][r]};
      *(LAS f32x4*)(p + 8 * r + 4) = f32x4{acc[4][r], acc[5][r], acc[6][r], acc[7][r]};
    }
  }
}

#define STAMP(k) do {} while (0)
#define STAMP_DECL() do {} while (0)
#define STAMP_FLUSH() do {} while (0)

__global__ void __launch_bounds__(512, 2) fused_kernel(Params p) {
  extern __shared__ __attribute__((aligned(16))) unsigned char smem[];
  LAS unsigned char* L = (LAS unsigned char*)smem;
  const int tid = threadIdx.x, lane = tid & 63, w = __builtin_amdgcn_readfirstlane(tid >> 6);
  const int g = blockIdx.x, xcd = g & 7, i = g >> 3;
  const int lg = lane >> 4, li = lane & 15;
  STAMP_DECL();
  STAMP(0);
  const unsigned tag = *(const unsigned*)(p.ws + WS_EPOCH) + 0x01010101u;
  const __amdgpu_buffer_rsrc_t rws = RSRC(p.ws, WS_END);

  const bool isP = i < 16;
  const int mi = i - 16, midx = xcd * 14 + mi;
  const bool isM = !isP && mi < 14;
  const int hP = xcd * 4 + (i >> 2), subP = i & 3;
  const int fb = midx >> 2, subM = midx & 3;
  const int blk = isP ? hP : fb, sub = isP ? subP : subM;
  const int grp = xcd * 4 + (i >> 3), split = i & 7;
  const int ab = grp >> 2, kvh = grp & 3;

  v4u xr[2];
  {
    const __amdgpu_buffer_rsrc_t rx = RSRC(p.x, 8 * 1024 * 2);
    xr[0] = __builtin_amdgcn_raw_buffer_load_b128(rx, lane * 16, w * 2048, 0);
    xr[1] = __builtin_amdgcn_raw_buffer_load_b128(rx, lane * 16 + 1024, w * 2048, 0);
  }
  v4u t1[8];
  {
    const int ld2 = isP ? 8192 : 7168;
    const __amdgpu_buffer_rsrc_t r1 = RSRC(isP ? p.wq : p.w1, isP ? 1024 * 4096 * 2 : (isM ? 1024 * 3584 * 2 : 0));
    const int voff = lg * 8 * ld2 + li * 16, soff = (sub * 256 + w * 32) * ld2 + blk * 256;
#pragma unroll
    for (int r = 0; r < 8; ++r) t1[r] = __builtin_amdgcn_raw_buffer_load_b128(r1, voff, soff + r * ld2, W_AUX);
  }
  v4u kr[4], vr[4];
  {
    const __amdgpu_buffer_rsrc_t rk = RSRC(p.kc, 8 * 4 * 4096 * 128 * 2), rv = RSRC(p.vc, 8 * 4 * 4096 * 128 * 2);
    const int soff = (grp * 4096 + 3072 + split * 128 + w * 16) * 256;
#pragma unroll
    for (int t = 0; t < 4; ++t) kr[t] = __builtin_amdgcn_raw_buffer_load_b128(rk, li * 256 + lg * 16 + t * 64, soff, W_AUX);
#pragma unroll
    for (int r = 0; r < 4; ++r) vr[r] = __builtin_amdgcn_raw_buffer_load_b128(rv, lg * 1024 + li * 16 + r * 256, soff, W_AUX);
  }
  SB();

  {
    float v[16];
#pragma unroll
    for (int j = 0; j < 4; ++j) { v[2 * j] = bf_lo(xr[0][j]); v[2 * j + 1] = bf_hi(xr[0][j]); v[8 + 2 * j] = bf_lo(xr[1][j]); v[9 + 2 * j] = bf_hi(xr[1][j]); }
    float s = 0.f;
#pragma unroll
    for (int j = 0; j < 16; ++j) s += v[j];
    const float mean = wave_sum(s) * (1.f / 1024.f);
    float ss = 0.f;
#pragma unroll
    for (int j = 0; j < 16; ++j) { const float d = v[j] - mean; ss = fmaf(d, d, ss); }
    const float var = wave_sum(ss) * (1.f / 1024.f);
    const float m = rbf(mean), den = rbf(sqrtf(rbf(rbf(var) + 1e-5f)));
    const float rc = 1.f / den;
    const bool hi = (sub & 2) != 0;
    const v4u xs = hi ? xr[1] : xr[0];
    unsigned o[4];
#pragma unroll
    for (int j = 0; j < 4; ++j) {
      float a0 = rbf(bf_lo(xs[j]) - m), a1 = rbf(bf_hi(xs[j]) - m);
      float q0 = a0 * rc, q1 = a1 * rc;
      q0 = fmaf(fmaf(-q0, den, a0), rc, q0); q1 = fmaf(fmaf(-q1, den, a1), rc, q1);
      o[j] = pk_bf16(q0, q1);
    }
    if ((lane >> 5) == (sub & 1)) *(LAS v4u*)(L + L_XS0 + w * 528 + (lane & 31) * 16) = v4u{o[0], o[1], o[2], o[3]};
  }
  asm volatile("" ::"s"(tag));
  STAMP(1);
  __syncthreads();

  {
    const v4u bf = *(const LAS v4u*)(L + L_XS0 + (li & 7) * 528 + w * 64 + lg * 16);
    tile_mfma_store(t1, bf, (LAS float*)(L + L_RED) + w * (8 * 132), lane);
  }
  STAMP(2);
  __syncthreads();
  if (tid < 256 && (isP || isM)) {
    const int b = tid >> 5, n4 = tid & 31;
    const LAS float* r = (const LAS float*)(L + L_RED) + b * 132 + n4 * 4;
    f32x4 s = *(const LAS f32x4*)r;
#pragma unroll
    for (int ww = 1; ww < 8; ++ww) s += *(const LAS f32x4*)(r + ww * (8 * 132));
    const int off = isP ? (int)WS_QP + ((sub * 8 + b) * 4096 + blk * 128 + n4 * 4) * 4 : (int)WS_HP + ((sub * 8 + b) * 3584 + blk * 128 + n4 * 4) * 4;
    __builtin_amdgcn_raw_buffer_store_b128(tag4(s, tag), rws, off, 0, SC1);
  }
  STAMP(3);

  asm volatile("" ::"v"(kr[3]));
  SB();
  STAMP(4);
  const unsigned tag16 = (tid & 1) ? (tag >> 16) : (tag & 0xffffu);
  v2u qg[4], hg[4];
  const int qoff = (int)WS_QP + (ab * 4096 + kvh * 1024 + tid * 2) * 4;
#define ISSUE_QG() _Pragma("unroll") for (int ks = 0; ks < 4; ++ks) qg[ks] = __builtin_amdgcn_raw_buffer_load_b64(rws, qoff, ks * (8 * 4096 * 4), SC1)
  ISSUE_QG();
  const int hoff = (int)WS_HP + ((tid >> 6) * 3584 + fb * 128 + (tid & 63) * 2) * 4;
  const __amdgpu_buffer_rsrc_t rwsM = RSRC(p.ws, isM ? WS_END : 0);
#define ISSUE_HG() _Pragma("unroll") for (int ks = 0; ks < 4; ++ks) hg[ks] = __builtin_amdgcn_raw_buffer_load_b64(rwsM, hoff, ks * (8 * 3584 * 4), SC1)
  ISSUE_HG();
  unsigned b1w;
  {
    const __amdgpu_buffer_rsrc_t rb1 = RSRC(p.b1, isM ? 3584 * 2 : 0);
    b1w = __builtin_amdgcn_raw_buffer_load_b32(rb1, (fb * 128 + (tid & 63) * 2) * 2, 0, 0);
  }
  v4u t3[8];
  {
    const __amdgpu_buffer_rsrc_t r3 = RSRC(isP ? p.wo : p.w2, isP ? 4096 * 1024 * 2 : (isM ? 3584 * 1024 * 2 : 0));
    const int voff = lg * 8 * 2048 + li * 16, soff = (blk * 128 + (w & 3) * 32) * 2048 + sub * 512 + (w >> 2) * 256;
#pragma unroll
    for (int r = 0; r < 8; ++r) t3[r] = __builtin_amdgcn_raw_buffer_load_b128(r3, voff, soff + r * 2048, W_AUX);
  }
  SB();

#define QG_OK() ((bad2(qg[0], tag16) | bad2(qg[1], tag16) | bad2(qg[2], tag16) | bad2(qg[3], tag16)) == 0u)
  if (!__all(QG_OK())) {
    int spins = 0;
    do { __builtin_amdgcn_s_sleep(1); CB(); ISSUE_QG(); } while (!__all(QG_OK()) && ++spins < SPIN_CAP);
  }
  {
    const float q0 = (untag(qg[0].x) + untag(qg[1].x)) + (untag(qg[2].x) + untag(qg[3].x));
    const float q1 = (untag(qg[0].y) + untag(qg[1].y)) + (untag(qg[2].y) + untag(qg[3].y));
    *(LAS unsigned*)(L + L_QS + (tid >> 6) * 272 + (tid & 63) * 4) = pk_bf16(q0, q1);
  }
  STAMP(5);
  __syncthreads();

  float sc[4];
  {
    f32x4 d = {0.f, 0.f, 0.f, 0.f};
#pragma unroll
    for (int t = 0; t < 4; ++t) {
      const v4u qf = *(const LAS v4u*)(L + L_QS + (li & 7) * 272 + t * 64 + lg * 16);
      d = mfma32(kr[t], qf, d);
    }
    float m = -INFINITY;
#pragma unroll
    for (int r = 0; r < 4; ++r) { sc[r] = rbf(rbf(d[r]) * 0.08838834764831845f); m = fmaxf(m, sc[r]); }
    m = x16_max(m); m = x32_max(m);
    if (lane < 8) *(LAS float*)(L + L_MX + lane * 32 + w * 4) = m;
  }
  __syncthreads();
  float Mwg;
  {
    const f32x4 a = *(const LAS f32x4*)(L + L_MX + (li & 7) * 32), b = *(const LAS f32x4*)(L + L_MX + (li & 7) * 32 + 16);
    Mwg = fmaxf(fmaxf(fmaxf(a.x, a.y), fmaxf(a.z, a.w)), fmaxf(fmaxf(b.x, b.y), fmaxf(b.z, b.w)));
    float pr[4], ls = 0.f;
#pragma unroll
    for (int r = 0; r < 4; ++r) { pr[r] = __builtin_amdgcn_exp2f((sc[r] - Mwg) * 1.4426950408889634f); ls += pr[r]; }
    ls = x16_sum(ls); ls = x32_sum(ls);
    if (lane < 8) *(LAS float*)(L + L_LS + lane * 32 + w * 4) = ls;
    const v2u pb = {pk_bf16(pr[0], pr[1]), pk_bf16(pr[2], pr[3])};
    f32x4 acc[8];
#pragma unroll
    for (int c = 0; c < 8; ++c) {
      const unsigned sel = (c & 1) ? 0x07060302u : 0x05040100u;
      v2u a2;
      a2.x = __builtin_amdgcn_perm(vr[1][c >> 1], vr[0][c >> 1], sel);
      a2.y = __builtin_amdgcn_perm(vr[3][c >> 1], vr[2][c >> 1], sel);
      acc[c] = mfma16(a2, pb, f32x4{0.f, 0.f, 0.f, 0.f});
    }
    if (li < 8) {
      LAS float* q = (LAS float*)(L + L_RED + RED_BYTES) + w * (8 * 132) + li * 132 + lg * 32;
#pragma unroll
      for (int r = 0; r < 4; ++r) {
        *(LAS f32x4*)(q + 8 * r) = f32x4{acc[0][r], acc[1][r], acc[2][r], acc[3][r]};
        *(LAS f32x4*)(q + 8 * r + 4) = f32x4{acc[4][r], acc[5][r], acc[6][r], acc[7][r]};
      }
    }
  }
  STAMP(6);
  __syncthreads();
  if (tid < 256) {
    const int hd = tid >> 5, d4 = tid & 31;
    const LAS float* r = (const LAS float*)(L + L_RED + RED_BYTES) + hd * 132 + d4 * 4;
    f32x4 s = *(const LAS f32x4*)r;
#pragma unroll
    for (int ww = 1; ww < 8; ++ww) s += *(const LAS f32x4*)(r + ww * (8 * 132));
    const int rec = ((kvh * 8 + hd) * 8 + ab) * 8 + split;
    __builtin_amdgcn_raw_buffer_store_b128(tag4(s, tag), rws, (int)WS_OP + rec * 512 + d4 * 16, 0, SC1);
  } else if (tid < 264) {
    const int hd = tid - 256;
    const f32x4 a = *(const LAS f32x4*)(L + L_LS + hd * 32), b = *(const LAS f32x4*)(L + L_LS + hd * 32 + 16);
    const float l = ((a.x + a.y) + (a.z + a.w)) + ((b.x + b.y) + (b.z + b.w));
    const int rec = ((kvh * 8 + hd) * 8 + ab) * 8 + split;
    __builtin_amdgcn_raw_buffer_store_b128(tag4(f32x4{Mwg, l, Mwg, l}, tag), rws, (int)WS_ML + rec * 16, 0, SC1);
  }
  STAMP(7);

  asm volatile("" ::"v"(t3[5]));
  SB();
  STAMP(8);
  if (isP) {
    v4u og[4], ml;
    const int ooff = (int)WS_OP + ((hP * 8 + w) * 8 + (lane >> 5)) * 512 + (lane & 31) * 16;
    const int moff = (int)WS_ML + ((hP * 8 + w) * 8 + (lane & 7)) * 16;
#define ISSUE_OG() do { _Pragma("unroll") for (int j = 0; j < 4; ++j) og[j] = __builtin_amdgcn_raw_buffer_load_b128(rws, ooff, j * 1024, SC1); \
                        ml = __builtin_amdgcn_raw_buffer_load_b128(rws, moff, 0, SC1); } while (0)
#define OG_OK() ((bad4(og[0], tag) | bad4(og[1], tag) | bad4(og[2], tag) | bad4(og[3], tag) | bad4(ml, tag)) == 0u)
    ISSUE_OG();
    {
      int spins = 0;
      while (!__all(OG_OK()) && ++spins < SPIN_CAP) { __builtin_amdgcn_s_sleep(1); CB(); ISSUE_OG(); }
    }
    const float Ms = untag(ml.x), Ls = untag(ml.y);
    float M = Ms; M = fmaxf(M, dpp<0xB1>(M)); M = fmaxf(M, dpp<0x4E>(M)); M = fmaxf(M, dpp<0x141>(M));
    const float wgt = __builtin_amdgcn_exp2f((Ms - M) * 1.4426950408889634f);
    float Lt = wgt * Ls; Lt += dpp<0xB1>(Lt); Lt += dpp<0x4E>(Lt); Lt += dpp<0x141>(Lt);
    const float wn = wgt / Lt;
    f32x4 o = {0.f, 0.f, 0.f, 0.f};
#pragma unroll
    for (int j = 0; j < 4; ++j) {
      const float c0 = __builtin_bit_cast(float, __builtin_amdgcn_readlane(__builtin_bit_cast(int, wn), 2 * j));
      const float c1 = __builtin_bit_cast(float, __builtin_amdgcn_readlane(__builtin_bit_cast(int, wn), 2 * j + 1));
      const float c = (lane >> 5) ? c1 : c0;
      o.x = fmaf(c, untag(og[j].x), o.x); o.y = fmaf(c, untag(og[j].y), o.y); o.z = fmaf(c, untag(og[j].z), o.z); o.w = fmaf(c, untag(og[j].w), o.w);
    }
    o.x = x32_sum(o.x); o.y = x32_sum(o.y); o.z = x32_sum(o.z); o.w = x32_sum(o.w);
    if (lane < 32) *(LAS v2u*)(L + L_XS + w * 272 + lane * 8) = v2u{pk_bf16(o.x, o.y), pk_bf16(o.z, o.w)};
  } else if (isM) {
#define HG_OK() ((bad2(hg[0], tag16) | bad2(hg[1], tag16) | bad2(hg[2], tag16) | bad2(hg[3], tag16)) == 0u)
    if (!__all(HG_OK())) {
      int spins = 0;
      do { __builtin_amdgcn_s_sleep(1); CB(); ISSUE_HG(); } while (!__all(HG_OK()) && ++spins < SPIN_CAP);
    }
    float hv[2];
    hv[0] = (untag(hg[0].x) + untag(hg[1].x)) + (untag(hg[2].x) + untag(hg[3].x));
    hv[1] = (untag(hg[0].y) + untag(hg[1].y)) + (untag(hg[2].y) + untag(hg[3].y));
#pragma unroll
    for (int j = 0; j < 2; ++j) {
      const float v = rbf(rbf(hv[j]) + (j ? bf_hi(b1w) : bf_lo(b1w)));
      const float u = 0.7978845608028654f * (v + 0.044715f * v * v * v);
      const float e = __builtin_amdgcn_exp2f(u * 2.8853900817779268f);
      const float th = 1.f - 2.f * __builtin_amdgcn_rcpf(e + 1.f);
      hv[j] = 0.5f * v * (1.f + th);
    }
    *(LAS unsigned*)(L + L_XS + (tid >> 6) * 272 + (tid & 63) * 4) = pk_bf16(hv[0], hv[1]);
  }
  STAMP(9);
  __syncthreads();
  {
    const v4u bf = *(const LAS v4u*)(L + L_XS + (li & 7) * 272 + (w & 3) * 64 + lg * 16);
    tile_mfma_store(t3, bf, (LAS float*)(L + L_RED + 2 * RED_BYTES) + w * (8 * 132), lane);
  }
  STAMP(10);
  __syncthreads();
  if (isP || isM) {
    const int b = tid >> 6, n4 = tid & 63;
    const LAS float* r = (const LAS float*)(L + L_RED + 2 * RED_BYTES) + (n4 >> 5) * (4 * 8 * 132) + b * 132 + (n4 & 31) * 4;
    f32x4 s = *(const LAS f32x4*)r;
#pragma unroll
    for (int ww = 1; ww < 4; ++ww) s += *(const LAS f32x4*)(r + ww * (8 * 132));
    const int off = (int)(isP ? WS_SP : WS_YP) + ((blk * 8 + b) * 1024 + sub * 256 + n4 * 4) * 4;
    __builtin_amdgcn_raw_buffer_store_b128(tag4(s, tag), rws, off, 0, SC1);
  }
  STAMP(11);

  {
    const int part = tid >> 3, q4 = tid & 7;
    const bool act = part < 60;
    const int off = (part < 32 ? (int)WS_SP + part * (8 * 1024 * 4) : (int)WS_YP + (part - 32) * (8 * 1024 * 4)) + (xcd * 1024 + i * 32 + q4 * 4) * 4;
    const int offc = act ? off : (int)WS_END;
    unsigned xw = 0, b2w = 0;
    if (tid < 16) { xw = *(const unsigned*)(p.x + xcd * 1024 + i * 32 + tid * 2); b2w = *(const unsigned*)(p.b2 + i * 32 + tid * 2); }
    v4u f = __builtin_amdgcn_raw_buffer_load_b128(rws, offc, 0, SC1);
    int spins = 0;
    while (!__all((bad4(f, tag) == 0u) | !act) && ++spins < SPIN_CAP) { CB(); f = __builtin_amdgcn_raw_buffer_load_b128(rws, offc, 0, SC1); }
    f32x4 v = {untag(f.x), untag(f.y), untag(f.z), untag(f.w)};
#pragma unroll
    for (int j = 0; j < 4; ++j) { float a = v[j]; a += dpp<0x128>(a); a = x16_sum(a); v[j] = x32_sum(a); }
    if (lane < 8) *(LAS f32x4*)(L + L_FIN + w * 128 + lane * 16) = v;
    STAMP(12);
    __syncthreads();
    if (tid < 16) {
      float o2[2];
#pragma unroll
      for (int j = 0; j < 2; ++j) {
        const LAS float* r = (const LAS float*)(L + L_FIN) + tid * 2 + j;
        const float s = (r[0] + r[32]) + (r[64] + r[96]), y = (r[128] + r[160]) + (r[192] + r[224]);
        const float yy = rbf(rbf(y) + (j ? bf_hi(b2w) : bf_lo(b2w)));
        const float gt = rbf(yy / (1.f + __expf(-yy)));
        o2[j] = (j ? bf_hi(xw) : bf_lo(xw)) + rbf(rbf(s) * gt);
      }
      *(unsigned*)(p.out + xcd * 1024 + i * 32 + tid * 2) = pk_bf16(o2[0], o2[1]);
      if (g == 0 && tid == 0) __hip_atomic_store((unsigned*)(p.ws + WS_EPOCH), tag, __ATOMIC_RELAXED, __HIP_MEMORY_SCOPE_AGENT);
    }
  }
  STAMP(13);
  STAMP_FLUSH();
}

extern "C" void kernel_launch(void* const* d_in, const int* in_sizes, int n_in, void* d_out, int out_size, void* d_ws, size_t ws_size,
                              hipStream_t stream) {
  Params p;
  p.x = (const u16*)d_in[0]; p.kc = (const u16*)d_in[1]; p.vc = (const u16*)d_in[2]; p.wq = (const u16*)d_in[3]; p.wo = (const u16*)d_in[4];
  p.w1 = (const u16*)d_in[6]; p.b1 = (const u16*)d_in[7]; p.w2 = (const u16*)d_in[8]; p.b2 = (const u16*)d_in[9];
  p.out = (u16*)d_out; p.ws = (unsigned char*)d_ws;
  constexpr int LDS = L_BYTES > 84 * 1024 ? L_BYTES : 84 * 1024;
  (void)hipFuncSetAttribute((const void*)fused_kernel, hipFuncAttributeMaxDynamicSharedMemorySize, LDS);
  hipLaunchKernelGGL(fused_kernel, dim3(256), dim3(512), LDS, stream, p);
}
```

```cpp
#include <hip/hip_runtime.h>
#include <hip/hip_bf16.h>
#include <stdint.h>

typedef unsigned v4u __attribute__((ext_vector_type(4)));
typedef unsigned v2u __attribute__((ext_vector_type(2)));
typedef float f32x4 __attribute__((ext_vector_type(4)));
typedef __bf16 bf16x8 __attribute__((ext_vector_type(8)));
typedef unsigned short u16;
#define LAS __attribute__((address_space(3)))

#define W_AUX 2
#define SC1 16
#define W1_AT 2
#define Y_DELAY 0
#define XA 3
#define XB 3
#define YA 3
#define WO_AT 0
#define OG_AT 3
#define QG_AT 3
#define W2_AT 0
#define HG_AT 3
#define SPIN_CAP 20000

constexpr unsigned WS_EPOCH = 0;
constexpr unsigned WS_QP = 4096;
constexpr unsigned WS_HP = WS_QP + 4 * 8 * 4096 * 4;
constexpr unsigned WS_OP = WS_HP + 4 * 8 * 3584 * 4;
constexpr unsigned WS_ML = WS_OP + 32 * 8 * 4 * 128 * 4;
constexpr unsigned WS_SP = WS_ML + 32 * 8 * 4 * 16;
constexpr unsigned WS_YP = WS_SP + 32 * 8 * 1024 * 4;
constexpr unsigned WS_END = WS_YP + 28 * 8 * 1024 * 4;
constexpr unsigned WS_STAMP = 8u << 20;

constexpr int L_XS0 = 0;
constexpr int L_XS = L_XS0 + 8 * 528;
constexpr int L_QS = L_XS + 8 * 272;
constexpr int L_MX = L_QS + 8 * 272;
constexpr int L_LS = L_MX + 256;
constexpr int L_FIN = L_LS + 256;
constexpr int L_RED = L_FIN + 1024;
constexpr int RED_BYTES = 8 * 8 * 132 * 4;
constexpr int L_KIMG = L_RED + RED_BYTES;
constexpr int L_BYTES = L_KIMG + 65536;

struct Params {
  const u16 *x, *kc, *vc, *wq, *wo, *w1, *b1, *w2, *b2;
  u16* out;
  unsigned char* ws;
};

__device__ __forceinline__ float bf_lo(unsigned w) { return __uint_as_float(w << 16); }
__device__ __forceinline__ float bf_hi(unsigned w) { return __uint_as_float(w & 0xffff0000u); }
__device__ __forceinline__ unsigned pk_bf16(float lo, float hi) {
  typedef __bf16 bf2 __attribute__((ext_vector_type(2)));
  typedef float f2 __attribute__((ext_vector_type(2)));
  f2 v = {lo, hi};
  return __builtin_bit_cast(unsigned, __builtin_convertvector(v, bf2));
}
__device__ __forceinline__ float rbf(float f) { return bf_lo(pk_bf16(f, f)); }
template <int CTRL> __device__ __forceinline__ float dpp(float x) {
  return __builtin_bit_cast(float, __builtin_amdgcn_mov_dpp(__builtin_bit_cast(int, x), CTRL, 0xf, 0xf, true));
}
__device__ __forceinline__ float x16_sum(float x) {
  auto s = __builtin_amdgcn_permlane16_swap(__float_as_uint(x), __float_as_uint(x), false, false);
  return __uint_as_float(s[0]) + __uint_as_float(s[1]);
}
__device__ __forceinline__ float x32_sum(float x) {
  auto s = __builtin_amdgcn_permlane32_swap(__float_as_uint(x), __float_as_uint(x), false, false);
  return __uint_as_float(s[0]) + __uint_as_float(s[1]);
}
__device__ __forceinline__ float x16_max(float x) {
  auto s = __builtin_amdgcn_permlane16_swap(__float_as_uint(x), __float_as_uint(x), false, false);
  return fmaxf(__uint_as_float(s[0]), __uint_as_float(s[1]));
}
__device__ __forceinline__ float x32_max(float x) {
  auto s = __builtin_amdgcn_permlane32_swap(__float_as_uint(x), __float_as_uint(x), false, false);
  return fmaxf(__uint_as_float(s[0]), __uint_as_float(s[1]));
}
__device__ __forceinline__ float wave_sum(float v) {
  v += dpp<0xB1>(v); v += dpp<0x4E>(v); v += dpp<0x141>(v); v += dpp<0x140>(v);
  v = x16_sum(v); return x32_sum(v);
}
__device__ __forceinline__ unsigned tagw(float v, unsigned tb) { return ((__float_as_uint(v) + 0x80u) & 0xFFFFFF00u) | tb; }
__device__ __forceinline__ float untag(unsigned w) { return __uint_as_float(w & 0xFFFFFF00u); }
__device__ __forceinline__ v4u tag4(f32x4 v, unsigned tag) {
  v4u o; o.x = tagw(v.x, tag & 0xff); o.y = tagw(v.y, (tag >> 8) & 0xff); o.z = tagw(v.z, (tag >> 16) & 0xff); o.w = tagw(v.w, tag >> 24); return o;
}
__device__ __forceinline__ unsigned bad4(v4u u, unsigned tag) {
  const unsigned lo = __builtin_amdgcn_perm(u.y, u.x, 0x0c0c0400u), hi = __builtin_amdgcn_perm(u.w, u.z, 0x04000c0cu);
  return (lo | hi) ^ tag;
}
__device__ __forceinline__ unsigned bad2(v2u u, unsigned tag16) { return __builtin_amdgcn_perm(u.y, u.x, 0x0c0c0400u) ^ tag16; }
__device__ __forceinline__ f32x4 mfma32(v4u a, v4u b, f32x4 c) {
  return __builtin_amdgcn_mfma_f32_16x16x32_bf16(__builtin_bit_cast(bf16x8, a), __builtin_bit_cast(bf16x8, b), c, 0, 0, 0);
}
#define RSRC(p, n) __builtin_amdgcn_make_buffer_rsrc((void*)(p), 0, (n), 0x00020000)
#define SB() __builtin_amdgcn_sched_barrier(0)
#define CB() asm volatile("" ::: "memory")
#define WAITV(x) do { asm volatile("" ::"v"(x)); SB(); } while (0)

__device__ __forceinline__ void tile_mfma_store(const v4u (&t)[8], v4u bfrag, LAS float* red, int lane) {
  f32x4 acc[8];
#pragma unroll
  for (int c = 0; c < 8; ++c) {
    const unsigned sel = (c & 1) ? 0x07060302u : 0x05040100u;
    v4u a;
    a.x = __builtin_amdgcn_perm(t[1][c >> 1], t[0][c >> 1], sel);
    a.y = __builtin_amdgcn_perm(t[3][c >> 1], t[2][c >> 1], sel);
    a.z = __builtin_amdgcn_perm(t[5][c >> 1], t[4][c >> 1], sel);
    a.w = __builtin_amdgcn_perm(t[7][c >> 1], t[6][c >> 1], sel);
    acc[c] = mfma32(a, bfrag, f32x4{0.f, 0.f, 0.f, 0.f});
  }
  const bool up = (lane & 8) != 0;
  LAS float* p = red + (lane & 7) * 132 + (lane >> 4) * 32 + (up ? 4 : 0);
#pragma unroll
  for (int r = 0; r < 4; ++r)
    *(LAS f32x4*)(p + 8 * r) = f32x4{up ? acc[4][r] : acc[0][r], up ? acc[5][r] : acc[1][r], up ? acc[6][r] : acc[2][r], up ? acc[7][r] : acc[3][r]};
}

#define STAMP(k) do {} while (0)
#define STAMP_DECL() do {} while (0)
#define STAMP_FLUSH() do {} while (0)

__global__ void __launch_bounds__(512, 2) fused_kernel(Params p) {
  extern __shared__ __attribute__((aligned(16))) unsigned char smem[];
  LAS unsigned char* L = (LAS unsigned char*)smem;
  const int tid = threadIdx.x, lane = tid & 63, w = __builtin_amdgcn_readfirstlane(tid >> 6);
  const int g = blockIdx.x, xcd = g & 7, i = g >> 3;
  const int lg = lane >> 4, li = lane & 15;
  STAMP_DECL();
  STAMP(0);
  const unsigned tag = *(const unsigned*)(p.ws + WS_EPOCH) + 0x01010101u;
  const __amdgpu_buffer_rsrc_t rws = RSRC(p.ws, WS_END);
  const int j = i & 15, jh = j >> 2, sub = j & 3;
  const int blk = xcd * 4 + jh;
  const int fb = jh * 8 + xcd;
  const bool hasM = fb < 28;
  LAS float* red0 = (LAS float*)(L + L_RED);
  LAS float* red1 = (LAS float*)(L + L_RED + RED_BYTES);

  if (i < 16) {
    v4u xr[2];
    {
      const __amdgpu_buffer_rsrc_t rx = RSRC(p.x, 8 * 1024 * 2);
      xr[0] = __builtin_amdgcn_raw_buffer_load_b128(rx, lane * 16, w * 2048, 0);
      xr[1] = __builtin_amdgcn_raw_buffer_load_b128(rx, lane * 16 + 1024, w * 2048, 0);
    }
    SB();
    __builtin_amdgcn_s_barrier();
    v4u ta[8], tb[8], tc[8];
    const __amdgpu_buffer_rsrc_t rwq = RSRC(p.wq, 1024 * 4096 * 2);
    const __amdgpu_buffer_rsrc_t rw1 = RSRC(p.w1, hasM ? 1024 * 3584 * 2 : 0);
    const __amdgpu_buffer_rsrc_t rwo = RSRC(p.wo, 4096 * 1024 * 2);
#define ISSUE_WQ(lo, hi) do { const int voff = lg * 8 * 8192 + li * 16, soff = (sub * 256 + w * 32) * 8192 + blk * 256; \
      _Pragma("unroll") for (int r = lo; r < hi; ++r) ta[r] = __builtin_amdgcn_raw_buffer_load_b128(rwq, voff, soff + r * 8192, W_AUX); SB(); } while (0)
#define ISSUE_W1(lo, hi) do { const int voff = lg * 8 * 7168 + li * 16, soff = (sub * 256 + w * 32) * 7168 + fb * 256; \
      _Pragma("unroll") for (int r = lo; r < hi; ++r) tb[r] = __builtin_amdgcn_raw_buffer_load_b128(rw1, voff, soff + r * 7168, W_AUX); SB(); } while (0)
#define ISSUE_WO(lo, hi) do { const int voff = lg * 8 * 2048 + li * 16, soff = (blk * 128 + (w & 3) * 32) * 2048 + sub * 512 + (w >> 2) * 256; \
      _Pragma("unroll") for (int r = lo; r < hi; ++r) tc[r] = __builtin_amdgcn_raw_buffer_load_b128(rwo, voff, soff + r * 2048, W_AUX); SB(); } while (0)
    ISSUE_WQ(0, 4);
    STAMP(14);
    WAITV(xr[1]);
    STAMP(10);
    ISSUE_WQ(4, 8);
    {
      float v[16];
#pragma unroll
      for (int q = 0; q < 4; ++q) { v[2 * q] = bf_lo(xr[0][q]); v[2 * q + 1] = bf_hi(xr[0][q]); v[8 + 2 * q] = bf_lo(xr[1][q]); v[9 + 2 * q] = bf_hi(xr[1][q]); }
      float s = 0.f;
#pragma unroll
      for (int q = 0; q < 16; ++q) s += v[q];
      const float mean = wave_sum(s) * (1.f / 1024.f);
      float ss = 0.f;
#pragma unroll
      for (int q = 0; q < 16; ++q) { const float d = v[q] - mean; ss = fmaf(d, d, ss); }
      const float var = wave_sum(ss) * (1.f / 1024.f);
      const float m = rbf(mean), den = rbf(sqrtf(rbf(rbf(var) + 1e-5f)));
      const float rc = 1.f / den;
      const v4u xs = (sub & 2) ? xr[1] : xr[0];
      unsigned o[4];
#pragma unroll
      for (int q = 0; q < 4; ++q) {
        const float a0 = rbf(bf_lo(xs[q]) - m), a1 = rbf(bf_hi(xs[q]) - m);
        float q0 = a0 * rc, q1 = a1 * rc;
        q0 = fmaf(fmaf(-q0, den, a0), rc, q0); q1 = fmaf(fmaf(-q1, den, a1), rc, q1);
        o[q] = pk_bf16(q0, q1);
      }
      if ((lane >> 5) == (sub & 1)) *(LAS v4u*)(L + L_XS0 + w * 528 + (lane & 31) * 16) = v4u{o[0], o[1], o[2], o[3]};
    }
    STAMP(11);
    asm volatile("" ::"s"(tag));
    STAMP(1);
    __syncthreads();
    STAMP(15);
    const v4u bf = *(const LAS v4u*)(L + L_XS0 + (li & 7) * 528 + w * 64 + lg * 16);
    WAITV(ta[XA]);
    STAMP(16);
    ISSUE_W1(0, 4);
    WAITV(ta[7]);
    STAMP(17);
    ISSUE_W1(4, 8);
    tile_mfma_store(ta, bf, red0 + w * (8 * 132), lane);
    STAMP(2);
    __syncthreads();
    if (tid < 256) {
      const int b = tid >> 5, n4 = tid & 31;
      const LAS float* r = red0 + b * 132 + n4 * 4;
      f32x4 s = *(const LAS f32x4*)r;
#pragma unroll
      for (int ww = 1; ww < 8; ++ww) s += *(const LAS f32x4*)(r + ww * (8 * 132));
      __builtin_amdgcn_raw_buffer_store_b128(tag4(s, tag), rws, (int)WS_QP + ((sub * 8 + b) * 4096 + blk * 128 + n4 * 4) * 4, 0, SC1);
    }
    STAMP(3);
    WAITV(tb[XB]);
    ISSUE_WO(0, 4);
    WAITV(tb[7]);
    ISSUE_WO(4, 8);
    tile_mfma_store(tb, bf, red1 + w * (8 * 132), lane);
    STAMP(4);
    __syncthreads();
    if (tid < 256 && hasM) {
      const int b = tid >> 5, n4 = tid & 31;
      const LAS float* r = red1 + b * 132 + n4 * 4;
      f32x4 s = *(const LAS f32x4*)r;
#pragma unroll
      for (int ww = 1; ww < 8; ++ww) s += *(const LAS f32x4*)(r + ww * (8 * 132));
      __builtin_amdgcn_raw_buffer_store_b128(tag4(s, tag), rws, (int)WS_HP + ((sub * 8 + b) * 3584 + fb * 128 + n4 * 4) * 4, 0, SC1);
    }
    STAMP(5);
    WAITV(tc[OG_AT]);
    STAMP(6);
    {
      v4u og[2], ml;
      const int ooff = (int)WS_OP + ((blk * 8 + w) * 4 + (lane >> 5)) * 512 + (lane & 31) * 16;
      const int moff = (int)WS_ML + ((blk * 8 + w) * 4 + (lane & 3)) * 16;
#define ISSUE_OG() do { og[0] = __builtin_amdgcn_raw_buffer_load_b128(rws, ooff, 0, SC1); og[1] = __builtin_amdgcn_raw_buffer_load_b128(rws, ooff, 1024, SC1); \
                        ml = __builtin_amdgcn_raw_buffer_load_b128(rws, moff, 0, SC1); } while (0)
#define OG_OK() ((bad4(og[0], tag) | bad4(og[1], tag) | bad4(ml, tag)) == 0u)
      ISSUE_OG();
      int spins = 0;
      while (!__all(OG_OK()) && ++spins < SPIN_CAP) { CB(); ISSUE_OG(); }
      const float Ms = untag(ml.x), Ls = untag(ml.y);
      float M = Ms; M = fmaxf(M, dpp<0xB1>(M)); M = fmaxf(M, dpp<0x4E>(M));
      const float wgt = __builtin_amdgcn_exp2f((Ms - M) * 1.4426950408889634f);
      float Lt = wgt * Ls; Lt += dpp<0xB1>(Lt); Lt += dpp<0x4E>(Lt);
      const float wn = wgt / Lt;
      f32x4 o = {0.f, 0.f, 0.f, 0.f};
#pragma unroll
      for (int q = 0; q < 2; ++q) {
        const float c0 = __builtin_bit_cast(float, __builtin_amdgcn_readlane(__builtin_bit_cast(int, wn), 2 * q));
        const float c1 = __builtin_bit_cast(float, __builtin_amdgcn_readlane(__builtin_bit_cast(int, wn), 2 * q + 1));
        const float c = (lane >> 5) ? c1 : c0;
        o.x = fmaf(c, untag(og[q].x), o.x); o.y = fmaf(c, untag(og[q].y), o.y); o.z = fmaf(c, untag(og[q].z), o.z); o.w = fmaf(c, untag(og[q].w), o.w);
      }
      o.x = x32_sum(o.x); o.y = x32_sum(o.y); o.z = x32_sum(o.z); o.w = x32_sum(o.w);
      if (lane < 32) *(LAS v2u*)(L + L_XS + w * 272 + lane * 8) = v2u{pk_bf16(o.x, o.y), pk_bf16(o.z, o.w)};
    }
    STAMP(7);
    __syncthreads();
    {
      const v4u bf3 = *(const LAS v4u*)(L + L_XS + (li & 7) * 272 + (w & 3) * 64 + lg * 16);
      tile_mfma_store(tc, bf3, red0 + w * (8 * 132), lane);
    }
    STAMP(8);
    __syncthreads();
    {
      const int b = tid >> 6, n4 = tid & 63;
      const LAS float* r = red0 + (n4 >> 5) * (4 * 8 * 132) + b * 132 + (n4 & 31) * 4;
      f32x4 s = *(const LAS f32x4*)r;
#pragma unroll
      for (int ww = 1; ww < 4; ++ww) s += *(const LAS f32x4*)(r + ww * (8 * 132));
      __builtin_amdgcn_raw_buffer_store_b128(tag4(s, tag), rws, (int)WS_SP + ((blk * 8 + b) * 1024 + sub * 256 + n4 * 4) * 4, 0, SC1);
    }
    STAMP(9);
  } else {
    const int ab = blk >> 2, kvh = blk & 3;
    v4u kr[8], vr[8], tw[8];
    const __amdgpu_buffer_rsrc_t rk = RSRC(p.kc, 8 * 4 * 4096 * 128 * 2), rv = RSRC(p.vc, 8 * 4 * 4096 * 128 * 2);
    const int kvoff = (blk * 4096 + 3072 + sub * 256 + w * 32) * 256;
#define ISSUE_K(lo, hi) do { _Pragma("unroll") for (int r = lo; r < hi; ++r) kr[r] = __builtin_amdgcn_raw_buffer_load_b128(rk, lg * 1024 + li * 16 + (r & 3) * 256 + (r >> 2) * 4096, kvoff, W_AUX); SB(); } while (0)
#define ISSUE_V(lo, hi) do { _Pragma("unroll") for (int r = lo; r < hi; ++r) vr[r] = __builtin_amdgcn_raw_buffer_load_b128(rv, lg * 1024 + li * 16 + (r & 3) * 256 + (r >> 2) * 4096, kvoff, W_AUX); SB(); } while (0)
    ISSUE_K(0, 4);
    WAITV(kr[YA]);
    ISSUE_K(4, 8);
    WAITV(kr[4 + YA]);
    ISSUE_V(0, 4);
    WAITV(kr[7]);
    ISSUE_V(4, 8);
    asm volatile("" ::"s"(tag));
    {
      LAS unsigned char* kimg = L + L_KIMG + w * 8192;
#pragma unroll
      for (int r = 0; r < 8; ++r) {
        const int key = 4 * lg + (r & 3) + 16 * (r >> 2);
        *(LAS v4u*)(kimg + key * 256 + ((li ^ (key & 15)) << 4)) = kr[r];
      }
#pragma unroll
      for (int t = 0; t < 8; ++t) kr[t] = *(const LAS v4u*)(kimg + (li + 16 * (t >> 2)) * 256 + (((4 * (t & 3) + lg) ^ li) << 4));
    }
    const unsigned tag16 = (tid & 1) ? (tag >> 16) : (tag & 0xffffu);
    unsigned b1w;
    {
      const __amdgpu_buffer_rsrc_t rb1 = RSRC(p.b1, hasM ? 3584 * 2 : 0);
      b1w = __builtin_amdgcn_raw_buffer_load_b32(rb1, (fb * 128 + (tid & 63) * 2) * 2, 0, 0);
    }
    const __amdgpu_buffer_rsrc_t rw2 = RSRC(p.w2, hasM ? 3584 * 1024 * 2 : 0);
#define ISSUE_W2(lo, hi) do { const int voff = lg * 8 * 2048 + li * 16, soff = (fb * 128 + (w & 3) * 32) * 2048 + sub * 512 + (w >> 2) * 256; \
      _Pragma("unroll") for (int r = lo; r < hi; ++r) tw[r] = __builtin_amdgcn_raw_buffer_load_b128(rw2, voff, soff + r * 2048, W_AUX); SB(); } while (0)
    WAITV(vr[QG_AT]);
    STAMP(1);
    {
      v2u qg[4];
      const int qoff = (int)WS_QP + (ab * 4096 + kvh * 1024 + tid * 2) * 4;
#define ISSUE_QG() _Pragma("unroll") for (int ks = 0; ks < 4; ++ks) qg[ks] = __builtin_amdgcn_raw_buffer_load_b64(rws, qoff, ks * (8 * 4096 * 4), SC1)
#define QG_OK() ((bad2(qg[0], tag16) | bad2(qg[1], tag16) | bad2(qg[2], tag16) | bad2(qg[3], tag16)) == 0u)
      ISSUE_QG();
      int spins = 0;
      while (!__all(QG_OK()) && ++spins < SPIN_CAP) { CB(); ISSUE_QG(); }
      const float q0 = (untag(qg[0].x) + untag(qg[1].x)) + (untag(qg[2].x) + untag(qg[3].x));
      const float q1 = (untag(qg[0].y) + untag(qg[1].y)) + (untag(qg[2].y) + untag(qg[3].y));
      *(LAS unsigned*)(L + L_QS + (tid >> 6) * 272 + (tid & 63) * 4) = pk_bf16(q0, q1);
    }
    ISSUE_W2(0, 4);
    STAMP(2);
    __syncthreads();
    float sc[8];
    {
      f32x4 d0 = {0.f, 0.f, 0.f, 0.f}, d1 = {0.f, 0.f, 0.f, 0.f};
#pragma unroll
      for (int t = 0; t < 4; ++t) {
        const v4u qf = *(const LAS v4u*)(L + L_QS + (li & 7) * 272 + t * 64 + lg * 16);
        d0 = mfma32(kr[t], qf, d0);
        d1 = mfma32(kr[4 + t], qf, d1);
      }
      float m = -INFINITY;
#pragma unroll
      for (int r = 0; r < 4; ++r) {
        sc[r] = rbf(rbf(d0[r]) * 0.08838834764831845f); sc[4 + r] = rbf(rbf(d1[r]) * 0.08838834764831845f);
        m = fmaxf(m, fmaxf(sc[r], sc[4 + r]));
      }
      m = x16_max(m); m = x32_max(m);
      if (lane < 8) *(LAS float*)(L + L_MX + lane * 32 + w * 4) = m;
    }
    STAMP(3);
    __syncthreads();
    float Mwg;
    {
      const f32x4 a = *(const LAS f32x4*)(L + L_MX + (li & 7) * 32), b = *(const LAS f32x4*)(L + L_MX + (li & 7) * 32 + 16);
      Mwg = fmaxf(fmaxf(fmaxf(a.x, a.y), fmaxf(a.z, a.w)), fmaxf(fmaxf(b.x, b.y), fmaxf(b.z, b.w)));
      float pr[8], ls = 0.f;
#pragma unroll
      for (int r = 0; r < 8; ++r) { pr[r] = __builtin_amdgcn_exp2f((sc[r] - Mwg) * 1.4426950408889634f); ls += pr[r]; }
      ls = x16_sum(ls); ls = x32_sum(ls);
      if (lane < 8) *(LAS float*)(L + L_LS + lane * 32 + w * 4) = ls;
      const v4u pb = {pk_bf16(pr[0], pr[1]), pk_bf16(pr[2], pr[3]), pk_bf16(pr[4], pr[5]), pk_bf16(pr[6], pr[7])};
      tile_mfma_store(vr, pb, red0 + w * (8 * 132), lane);
    }
    STAMP(4);
    __syncthreads();
    if (tid < 256) {
      const int hd = tid >> 5, d4 = tid & 31;
      const LAS float* r = red0 + hd * 132 + d4 * 4;
      f32x4 s = *(const LAS f32x4*)r;
#pragma unroll
      for (int ww = 1; ww < 8; ++ww) s += *(const LAS f32x4*)(r + ww * (8 * 132));
      const int rec = ((kvh * 8 + hd) * 8 + ab) * 4 + sub;
      __builtin_amdgcn_raw_buffer_store_b128(tag4(s, tag), rws, (int)WS_OP + rec * 512 + d4 * 16, 0, SC1);
    } else if (tid < 264) {
      const int hd = tid - 256;
      const f32x4 a = *(const LAS f32x4*)(L + L_LS + hd * 32), b = *(const LAS f32x4*)(L + L_LS + hd * 32 + 16);
      const float l = ((a.x + a.y) + (a.z + a.w)) + ((b.x + b.y) + (b.z + b.w));
      const int rec = ((kvh * 8 + hd) * 8 + ab) * 4 + sub;
      __builtin_amdgcn_raw_buffer_store_b128(tag4(f32x4{Mwg, l, Mwg, l}, tag), rws, (int)WS_ML + rec * 16, 0, SC1);
    }
    STAMP(5);
    ISSUE_W2(4, 8);
    WAITV(tw[HG_AT]);
    STAMP(6);
    if (hasM) {
      v2u hg[4];
      const int hoff = (int)WS_HP + ((tid >> 6) * 3584 + fb * 128 + (tid & 63) * 2) * 4;
#define ISSUE_HG() _Pragma("unroll") for (int ks = 0; ks < 4; ++ks) hg[ks] = __builtin_amdgcn_raw_buffer_load_b64(rws, hoff, ks * (8 * 3584 * 4), SC1)
#define HG_OK() ((bad2(hg[0], tag16) | bad2(hg[1], tag16) | bad2(hg[2], tag16) | bad2(hg[3], tag16)) == 0u)
      ISSUE_HG();
      int spins = 0;
      while (!__all(HG_OK()) && ++spins < SPIN_CAP) { CB(); ISSUE_HG(); }
      float hv[2];
      hv[0] = (untag(hg[0].x) + untag(hg[1].x)) + (untag(hg[2].x) + untag(hg[3].x));
      hv[1] = (untag(hg[0].y) + untag(hg[1].y)) + (untag(hg[2].y) + untag(hg[3].y));
#pragma unroll
      for (int q = 0; q < 2; ++q) {
        const float v = rbf(rbf(hv[q]) + (q ? bf_hi(b1w) : bf_lo(b1w)));
        const float u = 0.7978845608028654f * (v + 0.044715f * v * v * v);
        const float e = __builtin_amdgcn_exp2f(u * 2.8853900817779268f);
        const float th = 1.f - 2.f * __builtin_amdgcn_rcpf(e + 1.f);
        hv[q] = 0.5f * v * (1.f + th);
      }
      *(LAS unsigned*)(L + L_XS + (tid >> 6) * 272 + (tid & 63) * 4) = pk_bf16(hv[0], hv[1]);
    }
    STAMP(7);
    __syncthreads();
    {
      const v4u bf3 = *(const LAS v4u*)(L + L_XS + (li & 7) * 272 + (w & 3) * 64 + lg * 16);
      tile_mfma_store(tw, bf3, red1 + w * (8 * 132), lane);
    }
    STAMP(8);
    __syncthreads();
    if (hasM) {
      const int b = tid >> 6, n4 = tid & 63;
      const LAS float* r = red1 + (n4 >> 5) * (4 * 8 * 132) + b * 132 + (n4 & 31) * 4;
      f32x4 s = *(const LAS f32x4*)r;
#pragma unroll
      for (int ww = 1; ww < 4; ++ww) s += *(const LAS f32x4*)(r + ww * (8 * 132));
      __builtin_amdgcn_raw_buffer_store_b128(tag4(s, tag), rws, (int)WS_YP + ((fb * 8 + b) * 1024 + sub * 256 + n4 * 4) * 4, 0, SC1);
    }
    STAMP(9);
  }

  {
    const int part = tid >> 3, q4 = tid & 7;
    const bool act = part < 60;
    const int off = (part < 32 ? (int)WS_SP + part * (8 * 1024 * 4) : (int)WS_YP + (part - 32) * (8 * 1024 * 4)) + (xcd * 1024 + i * 32 + q4 * 4) * 4;
    const int offc = act ? off : (int)WS_END;
    u16 xw = 0, b2w = 0;
    if (tid < 32) { xw = p.x[xcd * 1024 + i * 32 + tid]; b2w = p.b2[i * 32 + tid]; }
    v4u f = __builtin_amdgcn_raw_buffer_load_b128(rws, offc, 0, SC1);
    int spins = 0;
    while (!__all((bad4(f, tag) == 0u) | !act) && ++spins < SPIN_CAP) { CB(); f = __builtin_amdgcn_raw_buffer_load_b128(rws, offc, 0, SC1); }
    f32x4 v = {untag(f.x), untag(f.y), untag(f.z), untag(f.w)};
#pragma unroll
    for (int q = 0; q < 4; ++q) { float a = v[q]; a += dpp<0x128>(a); a = x16_sum(a); v[q] = x32_sum(a); }
    if (lane < 8) *(LAS f32x4*)(L + L_FIN + w * 128 + lane * 16) = v;
    STAMP(12);
    __syncthreads();
    if (tid < 32) {
      const LAS float* r = (const LAS float*)(L + L_FIN) + tid;
      const float s = (r[0] + r[32]) + (r[64] + r[96]), y = (r[128] + r[160]) + (r[192] + r[224]);
      const float yy = rbf(rbf(y) + bf_lo(b2w));
      const float e = __builtin_amdgcn_exp2f(-yy * 1.4426950408889634f);
      const float gt = rbf(yy * __builtin_amdgcn_rcpf(1.f + e));
      const float o = bf_lo(xw) + rbf(rbf(s) * gt);
      p.out[xcd * 1024 + i * 32 + tid] = (u16)pk_bf16(o, o);
      if (g == 0 && tid == 0) __hip_atomic_store((unsigned*)(p.ws + WS_EPOCH), tag, __ATOMIC_RELAXED, __HIP_MEMORY_SCOPE_AGENT);
    }
  }
  STAMP(13);
  STAMP_FLUSH();
}

extern "C" void kernel_launch(void* const* d_in, const int* in_sizes, int n_in, void* d_out, int out_size, void* d_ws, size_t ws_size,
                              hipStream_t stream) {
  Params p;
  p.x = (const u16*)d_in[0]; p.kc = (const u16*)d_in[1]; p.vc = (const u16*)d_in[2]; p.wq = (const u16*)d_in[3]; p.wo = (const u16*)d_in[4];
  p.w1 = (const u16*)d_in[6]; p.b1 = (const u16*)d_in[7]; p.w2 = (const u16*)d_in[8]; p.b2 = (const u16*)d_in[9];
  p.out = (u16*)d_out; p.ws = (unsigned char*)d_ws;
  constexpr int LDS = L_BYTES > 84 * 1024 ? L_BYTES : 84 * 1024;
  (void)hipFuncSetAttribute((const void*)fused_kernel, hipFuncAttributeMaxDynamicSharedMemorySize, LDS);
  hipLaunchKernelGGL(fused_kernel, dim3(256), dim3(512), LDS, stream, p);
}
```

```cpp
#include <hip/hip_runtime.h>
#include <hip/hip_bf16.h>
#include <stdint.h>

typedef unsigned v4u __attribute__((ext_vector_type(4)));
typedef unsigned v2u __attribute__((ext_vector_type(2)));
typedef float f32x4 __attribute__((ext_vector_type(4)));
typedef __bf16 bf16x8 __attribute__((ext_vector_type(8)));
typedef short s16x4 __attribute__((ext_vector_type(4)));
typedef unsigned short u16;
#define LAS __attribute__((address_space(3)))

constexpr int W_AUX = 2;
constexpr int SC1 = 16;
constexpr int SPIN_CAP = 20000;
constexpr int PSLEEP = 2;
constexpr int X_DELAY = 420;
constexpr int YK1 = 3, YK2 = 8, YV1 = 4;
constexpr int XB = 3, OG_AT = 7, QG_AT = 5, HG_AT = 5;

constexpr unsigned WS_EPOCH = 0;
constexpr unsigned WS_QP = 4096;
constexpr unsigned WS_HP = WS_QP + 8 * 8 * 4096 * 4;
constexpr unsigned WS_OP = WS_HP + 4 * 8 * 3584 * 4;
constexpr unsigned WS_ML = WS_OP + 32 * 8 * 4 * 128 * 4;
constexpr unsigned WS_SP = WS_ML + 32 * 8 * 4 * 16;
constexpr unsigned WS_YP = WS_SP + 32 * 8 * 1024 * 4;
constexpr unsigned WS_END = WS_YP + 28 * 8 * 1024 * 4;

constexpr int L_XS0 = 0;
constexpr int L_XS = L_XS0 + 8 * 528;
constexpr int L_QS = L_XS + 8 * 272;
constexpr int L_MX = L_QS + 8 * 272;
constexpr int L_LS = L_MX + 256;
constexpr int L_FIN = L_LS + 256;
constexpr int L_RED = L_FIN + 1024;
constexpr int RED_BYTES = 8 * 8 * 132 * 4;
constexpr int L_KIMG = L_RED + RED_BYTES;
constexpr int L_BYTES = L_KIMG + 65536;

struct Params {
  const u16 *x, *kc, *vc, *wq, *wo, *w1, *b1, *w2, *b2;
  u16* out;
  unsigned char* ws;
};

__device__ __forceinline__ float bf_lo(unsigned w) { return __uint_as_float(w << 16); }
__device__ __forceinline__ float bf_hi(unsigned w) { return __uint_as_float(w & 0xffff0000u); }
__device__ __forceinline__ unsigned pk_bf16(float lo, float hi) {
  typedef __bf16 bf2 __attribute__((ext_vector_type(2)));
  typedef float f2 __attribute__((ext_vector_type(2)));
  f2 v = {lo, hi};
  return __builtin_bit_cast(unsigned, __builtin_convertvector(v, bf2));
}
__device__ __forceinline__ float rbf(float f) { return bf_lo(pk_bf16(f, f)); }
template <int CTRL> __device__ __forceinline__ float dpp(float x) {
  return __builtin_bit_cast(float, __builtin_amdgcn_mov_dpp(__builtin_bit_cast(int, x), CTRL, 0xf, 0xf, true));
}
__device__ __forceinline__ float x16_sum(float x) {
  auto s = __builtin_amdgcn_permlane16_swap(__float_as_uint(x), __float_as_uint(x), false, false);
  return __uint_as_float(s[0]) + __uint_as_float(s[1]);
}
__device__ __forceinline__ float x32_sum(float x) {
  auto s = __builtin_amdgcn_permlane32_swap(__float_as_uint(x), __float_as_uint(x), false, false);
  return __uint_as_float(s[0]) + __uint_as_float(s[1]);
}
__device__ __forceinline__ float vmax(float a, float b) { float r; asm("v_max_f32 %0, %1, %2" : "=v"(r) : "v"(a), "v"(b)); return r; }
__device__ __forceinline__ float vmax3(float a, float b, float c) { float r; asm("v_max3_f32 %0, %1, %2, %3" : "=v"(r) : "v"(a), "v"(b), "v"(c)); return r; }
__device__ __forceinline__ unsigned dppu8(unsigned x) { return (unsigned)__builtin_amdgcn_mov_dpp((int)x, 0x128, 0xf, 0xf, true); }
__device__ __forceinline__ float x16_max(float x) {
  auto s = __builtin_amdgcn_permlane16_swap(__float_as_uint(x), __float_as_uint(x), false, false);
  return vmax(__uint_as_float(s[0]), __uint_as_float(s[1]));
}
__device__ __forceinline__ float x32_max(float x) {
  auto s = __builtin_amdgcn_permlane32_swap(__float_as_uint(x), __float_as_uint(x), false, false);
  return vmax(__uint_as_float(s[0]), __uint_as_float(s[1]));
}
__device__ __forceinline__ float wave_sum(float v) {
  v += dpp<0xB1>(v); v += dpp<0x4E>(v); v += dpp<0x141>(v); v += dpp<0x140>(v);
  v = x16_sum(v); return x32_sum(v);
}
__device__ __forceinline__ unsigned tagw(float v, unsigned tb) { return ((__float_as_uint(v) + 0x80u) & 0xFFFFFF00u) | tb; }
__device__ __forceinline__ float untag(unsigned w) { return __uint_as_float(w & 0xFFFFFF00u); }
__device__ __forceinline__ v4u tag4(f32x4 v, unsigned tag) {
  v4u o; o.x = tagw(v.x, tag & 0xff); o.y = tagw(v.y, (tag >> 8) & 0xff); o.z = tagw(v.z, (tag >> 16) & 0xff); o.w = tagw(v.w, tag >> 24); return o;
}
__device__ __forceinline__ unsigned tagq(float v, unsigned tb) { return ((__float_as_uint(v) + 0x8u) & 0xFFFFFFF0u) | (tb & 0xFu); }
__device__ __forceinline__ float untagq(unsigned w) { return __uint_as_float(w & 0xFFFFFFF0u); }
__device__ __forceinline__ v4u tag4q(f32x4 v, unsigned tag) {
  v4u o; o.x = tagq(v.x, tag); o.y = tagq(v.y, tag >> 8); o.z = tagq(v.z, tag >> 16); o.w = tagq(v.w, tag >> 24); return o;
}
__device__ __forceinline__ unsigned bad2q(v2u u, unsigned tag16) { return (__builtin_amdgcn_perm(u.y, u.x, 0x0c0c0400u) ^ tag16) & 0x0F0Fu; }
__device__ __forceinline__ unsigned bad4(v4u u, unsigned tag) {
  const unsigned lo = __builtin_amdgcn_perm(u.y, u.x, 0x0c0c0400u), hi = __builtin_amdgcn_perm(u.w, u.z, 0x04000c0cu);
  return (lo | hi) ^ tag;
}
__device__ __forceinline__ unsigned bad2(v2u u, unsigned tag16) { return __builtin_amdgcn_perm(u.y, u.x, 0x0c0c0400u) ^ tag16; }
__device__ __forceinline__ f32x4 mfma32(v4u a, v4u b, f32x4 c) {
  return __builtin_amdgcn_mfma_f32_16x16x32_bf16(__builtin_bit_cast(bf16x8, a), __builtin_bit_cast(bf16x8, b), c, 0, 0, 0);
}
__device__ __forceinline__ f32x4 mfma16(v2u a, v2u b, f32x4 c) {
  return __builtin_amdgcn_mfma_f32_16x16x16bf16_1k(__builtin_bit_cast(s16x4, a), __builtin_bit_cast(s16x4, b), c, 0, 0, 0);
}
#define RSRC(p, n) __builtin_amdgcn_make_buffer_rsrc((void*)(p), 0, (n), 0x00020000)
#define SB() __builtin_amdgcn_sched_barrier(0)
#define CB() asm volatile("" ::: "memory")
#define WAITV(x) do { asm volatile("" ::"v"(x)); SB(); } while (0)

__device__ __forceinline__ void acc_store(const f32x4 (&acc)[8], LAS float* red, int lane) {
  const bool up = (lane & 8) != 0;
  LAS float* p = red + (lane & 7) * 132 + (lane >> 4) * 32 + (up ? 4 : 0);
#pragma unroll
  for (int r = 0; r < 4; ++r)
    *(LAS f32x4*)(p + 8 * r) = f32x4{up ? acc[4][r] : acc[0][r], up ? acc[5][r] : acc[1][r], up ? acc[6][r] : acc[2][r], up ? acc[7][r] : acc[3][r]};
}
__device__ __forceinline__ void tile_mfma_store(const v4u (&t)[8], v4u bfrag, LAS float* red, int lane) {
  f32x4 acc[8];
#pragma unroll
  for (int c = 0; c < 8; ++c) {
    const unsigned sel = (c & 1) ? 0x07060302u : 0x05040100u;
    v4u a;
    a.x = __builtin_amdgcn_perm(t[1][c >> 1], t[0][c >> 1], sel);
    a.y = __builtin_amdgcn_perm(t[3][c >> 1], t[2][c >> 1], sel);
    a.z = __builtin_amdgcn_perm(t[5][c >> 1], t[4][c >> 1], sel);
    a.w = __builtin_amdgcn_perm(t[7][c >> 1], t[6][c >> 1], sel);
    acc[c] = mfma32(a, bfrag, f32x4{0.f, 0.f, 0.f, 0.f});
  }
  acc_store(acc, red, lane);
}
__device__ __forceinline__ void tile_prep(v4u (&t)[8]) {
  v4u a[8];
#pragma unroll
  for (int c = 0; c < 8; ++c) {
    const unsigned sel = (c & 1) ? 0x07060302u : 0x05040100u;
    a[c].x = __builtin_amdgcn_perm(t[1][c >> 1], t[0][c >> 1], sel);
    a[c].y = __builtin_amdgcn_perm(t[3][c >> 1], t[2][c >> 1], sel);
    a[c].z = __builtin_amdgcn_perm(t[5][c >> 1], t[4][c >> 1], sel);
    a[c].w = __builtin_amdgcn_perm(t[7][c >> 1], t[6][c >> 1], sel);
  }
#pragma unroll
  for (int c = 0; c < 8; ++c) { t[c] = a[c]; asm volatile("" : "+v"(t[c])); }
}
__device__ __forceinline__ void prepped_mfma_store(const v4u (&a)[8], v4u bfrag, LAS float* red, int lane) {
  f32x4 acc[8];
#pragma unroll
  for (int c = 0; c < 8; ++c) acc[c] = mfma32(a[c], bfrag, f32x4{0.f, 0.f, 0.f, 0.f});
  acc_store(acc, red, lane);
}
__device__ __forceinline__ void half_mfma_store(const v4u (&t)[4], v2u bfrag, LAS float* red, int lane) {
  f32x4 acc[8];
#pragma unroll
  for (int c = 0; c < 8; ++c) {
    const unsigned sel = (c & 1) ? 0x07060302u : 0x05040100u;
    v2u a;
    a.x = __builtin_amdgcn_perm(t[1][c >> 1], t[0][c >> 1], sel);
    a.y = __builtin_amdgcn_perm(t[3][c >> 1], t[2][c >> 1], sel);
    acc[c] = mfma16(a, bfrag, f32x4{0.f, 0.f, 0.f, 0.f});
  }
  acc_store(acc, red, lane);
}


__global__ void __launch_bounds__(512, 2) fused_kernel(Params p) {
  extern __shared__ __attribute__((aligned(16))) unsigned char smem[];
  LAS unsigned char* L = (LAS unsigned char*)smem;
  const int tid = threadIdx.x, lane = tid & 63, w = __builtin_amdgcn_readfirstlane(tid >> 6);
  const int g = blockIdx.x, xcd = g & 7, i = g >> 3;
  const int lg = lane >> 4, li = lane & 15;
  const unsigned long long tstart = __builtin_amdgcn_s_memrealtime();
  const unsigned tag = *(const unsigned*)(p.ws + WS_EPOCH) + 0x01010101u;
  const __amdgpu_buffer_rsrc_t rws = RSRC(p.ws, WS_END);
  const bool isX = i < 16;
  const int j = i & 15, jh = j >> 2, sub = j & 3;
  const int blk = xcd * 4 + jh;
  const int fb = jh * 8 + xcd;
  const bool hasM = fb < 28;
  const int hq = 8 * (xcd >> 1) + (j >> 1), ks = 2 * (xcd & 1) + (j & 1), ks8 = 2 * ks + (isX ? 0 : 1);
  const bool hasM1 = hq < 28;
  LAS float* red0 = (LAS float*)(L + L_RED);
  LAS float* red1 = (LAS float*)(L + L_RED + RED_BYTES);

  v4u xr[2], tq[4];
  {
    const __amdgpu_buffer_rsrc_t rx = RSRC(p.x, 8 * 1024 * 2);
    xr[0] = __builtin_amdgcn_raw_buffer_load_b128(rx, lane * 16, w * 2048, 0);
    xr[1] = __builtin_amdgcn_raw_buffer_load_b128(rx, lane * 16 + 1024, w * 2048, 0);
    SB();
    __builtin_amdgcn_s_barrier();
    const __amdgpu_buffer_rsrc_t rwq = RSRC(p.wq, 1024 * 4096 * 2);
    const int voff = lg * 4 * 8192 + li * 16, soff = (ks8 * 128 + w * 16) * 8192 + hq * 256;
#pragma unroll
    for (int r = 0; r < 4; ++r) tq[r] = __builtin_amdgcn_raw_buffer_load_b128(rwq, voff, soff + r * 8192, W_AUX);
    SB();
  }
  v4u ta[8], tb[8], tc[8];
  const int kvoff = (blk * 4096 + 3072 + sub * 256 + w * 32) * 256;
  const __amdgpu_buffer_rsrc_t ra = RSRC(isX ? p.w1 : p.kc, isX ? (hasM1 ? 1024 * 3584 * 2 : 0) : 8 * 4 * 4096 * 128 * 2);
  const int a_v = isX ? lg * 8 * 7168 + li * 16 : lg * 1024 + li * 16, a_s = isX ? (ks * 256 + w * 32) * 7168 + hq * 256 : kvoff;
#define ISSUE_A(lo, hi) do { _Pragma("unroll") for (int r = lo; r < hi; ++r) \
      ta[r] = __builtin_amdgcn_raw_buffer_load_b128(ra, a_v, a_s + (isX ? r * 7168 : (r & 3) * 256 + (r >> 2) * 4096), W_AUX); SB(); } while (0)
  const __amdgpu_buffer_rsrc_t rb = RSRC(isX ? p.wo : p.vc, isX ? 4096 * 1024 * 2 : 8 * 4 * 4096 * 128 * 2);
  const int b_v = isX ? lg * 8 * 2048 + li * 16 : lg * 1024 + li * 16, b_s = isX ? (blk * 128 + (w & 3) * 32) * 2048 + sub * 512 + (w >> 2) * 256 : kvoff;
#define ISSUE_B(lo, hi) do { _Pragma("unroll") for (int r = lo; r < hi; ++r) \
      tb[r] = __builtin_amdgcn_raw_buffer_load_b128(rb, b_v, b_s + (isX ? r * 2048 : (r & 3) * 256 + (r >> 2) * 4096), W_AUX); SB(); } while (0)

  if (!isX) { WAITV(xr[1]); ISSUE_A(0, YK1); }
  {
    float v[16];
#pragma unroll
    for (int q = 0; q < 4; ++q) { v[2 * q] = bf_lo(xr[0][q]); v[2 * q + 1] = bf_hi(xr[0][q]); v[8 + 2 * q] = bf_lo(xr[1][q]); v[9 + 2 * q] = bf_hi(xr[1][q]); }
    float s = 0.f;
#pragma unroll
    for (int q = 0; q < 16; ++q) s += v[q];
    const float mean = wave_sum(s) * (1.f / 1024.f);
    float ss = 0.f;
#pragma unroll
    for (int q = 0; q < 16; ++q) { const float d = v[q] - mean; ss = fmaf(d, d, ss); }
    const float var = wave_sum(ss) * (1.f / 1024.f);
    const float m = rbf(mean), den = rbf(sqrtf(rbf(rbf(var) + 1e-5f)));
    const float rc = 1.f / den;
    const v4u xs = (ks & 2) ? xr[1] : xr[0];
    unsigned o[4];
#pragma unroll
    for (int q = 0; q < 4; ++q) {
      const float a0 = rbf(bf_lo(xs[q]) - m), a1 = rbf(bf_hi(xs[q]) - m);
      float q0 = a0 * rc, q1 = a1 * rc;
      q0 = fmaf(fmaf(-q0, den, a0), rc, q0); q1 = fmaf(fmaf(-q1, den, a1), rc, q1);
      o[q] = pk_bf16(q0, q1);
    }
    if ((lane >> 5) == (ks & 1)) *(LAS v4u*)(L + L_XS0 + w * 528 + (lane & 31) * 16) = v4u{o[0], o[1], o[2], o[3]};
  }
  asm volatile("" ::"s"(tag));
  if (!isX) ISSUE_A(YK1, YK2);
  __syncthreads();
  {
    const v2u bq = *(const LAS v2u*)(L + L_XS0 + (li & 7) * 528 + (isX ? 0 : 256) + w * 32 + lg * 8);
    half_mfma_store(tq, bq, red0 + w * (8 * 132), lane);
  }
  if (!isX) { ISSUE_A(YK2, 8); ISSUE_B(0, YV1); }
  __syncthreads();
  if (tid < 256) {
    const int b = tid >> 5, n4 = tid & 31;
    const LAS float* r = red0 + b * 132 + n4 * 4;
    f32x4 s = *(const LAS f32x4*)r;
#pragma unroll
    for (int ww = 1; ww < 8; ++ww) s += *(const LAS f32x4*)(r + ww * (8 * 132));
    __builtin_amdgcn_raw_buffer_store_b128(tag4q(s, tag), rws, (int)WS_QP + ((ks8 * 8 + b) * 4096 + hq * 128 + n4 * 4) * 4, 0, SC1);
  }

  if (isX) {
    { while ((long long)(__builtin_amdgcn_s_memrealtime() - tstart) < X_DELAY) __builtin_amdgcn_s_sleep(2); }
    ISSUE_A(0, 8);
    const v4u bf = *(const LAS v4u*)(L + L_XS0 + (li & 7) * 528 + w * 64 + lg * 16);
    WAITV(ta[XB]);
    ISSUE_B(0, 4);
    WAITV(ta[7]);
    ISSUE_B(4, 8);
    tile_mfma_store(ta, bf, red1 + w * (8 * 132), lane);
    __syncthreads();
    if (tid < 256 && hasM1) {
      const int b = tid >> 5, n4 = tid & 31;
      const LAS float* r = red1 + b * 132 + n4 * 4;
      f32x4 s = *(const LAS f32x4*)r;
#pragma unroll
      for (int ww = 1; ww < 8; ++ww) s += *(const LAS f32x4*)(r + ww * (8 * 132));
      __builtin_amdgcn_raw_buffer_store_b128(tag4(s, tag), rws, (int)WS_HP + ((ks * 8 + b) * 3584 + hq * 128 + n4 * 4) * 4, 0, SC1);
    }
    WAITV(tb[OG_AT]);
    {
      v4u og[2], ml;
      const int ooff = (int)WS_OP + ((blk * 8 + w) * 4 + (lane >> 5)) * 512 + (lane & 31) * 16;
      const int moff = (int)WS_ML + ((blk * 8 + w) * 4 + (lane & 3)) * 16;
#define ISSUE_OG() do { og[0] = __builtin_amdgcn_raw_buffer_load_b128(rws, ooff, 0, SC1); og[1] = __builtin_amdgcn_raw_buffer_load_b128(rws, ooff, 1024, SC1); \
                        ml = __builtin_amdgcn_raw_buffer_load_b128(rws, moff, 0, SC1); } while (0)
#define OG_OK() ((bad4(og[0], tag) | bad4(og[1], tag) | bad4(ml, tag)) == 0u)
      ISSUE_OG();
      SB();
      tile_prep(tb);
      SB();
      if (!__all(OG_OK())) {
        int spins = 0;
        do { __builtin_amdgcn_s_sleep(PSLEEP); CB(); ISSUE_OG(); } while (!__all(OG_OK()) && ++spins < SPIN_CAP);
      }
      const float Ms = untag(ml.x), Ls = untag(ml.y);
      float M = Ms; M = vmax(M, dpp<0xB1>(M)); M = vmax(M, dpp<0x4E>(M));
      const float wgt = __builtin_amdgcn_exp2f(Ms - M);
      float Lt = wgt * Ls; Lt += dpp<0xB1>(Lt); Lt += dpp<0x4E>(Lt);
      const float wn = wgt * __builtin_amdgcn_rcpf(Lt);
      f32x4 o = {0.f, 0.f, 0.f, 0.f};
#pragma unroll
      for (int q = 0; q < 2; ++q) {
        const float c0 = __builtin_bit_cast(float, __builtin_amdgcn_readlane(__builtin_bit_cast(int, wn), 2 * q));
        const float c1 = __builtin_bit_cast(float, __builtin_amdgcn_readlane(__builtin_bit_cast(int, wn), 2 * q + 1));
        const float c = (lane >> 5) ? c1 : c0;
        o.x = fmaf(c, untag(og[q].x), o.x); o.y = fmaf(c, untag(og[q].y), o.y); o.z = fmaf(c, untag(og[q].z), o.z); o.w = fmaf(c, untag(og[q].w), o.w);
      }
      o.x = x32_sum(o.x); o.y = x32_sum(o.y); o.z = x32_sum(o.z); o.w = x32_sum(o.w);
      if (lane < 32) *(LAS v2u*)(L + L_XS + w * 272 + lane * 8) = v2u{pk_bf16(o.x, o.y), pk_bf16(o.z, o.w)};
    }
    __syncthreads();
    {
      const v4u bf3 = *(const LAS v4u*)(L + L_XS + (li & 7) * 272 + (w & 3) * 64 + lg * 16);
      prepped_mfma_store(tb, bf3, red0 + w * (8 * 132), lane);
    }
    __syncthreads();
    {
      const int b = tid >> 6, n4 = tid & 63;
      const LAS float* r = red0 + (n4 >> 5) * (4 * 8 * 132) + b * 132 + (n4 & 31) * 4;
      f32x4 s = *(const LAS f32x4*)r;
#pragma unroll
      for (int ww = 1; ww < 4; ++ww) s += *(const LAS f32x4*)(r + ww * (8 * 132));
      __builtin_amdgcn_raw_buffer_store_b128(tag4(s, tag), rws, (int)WS_SP + ((blk * 8 + b) * 1024 + sub * 256 + n4 * 4) * 4, 0, SC1);
    }
  } else {
    const int ab = blk >> 2, kvh = blk & 3;
    ISSUE_B(YV1, 4);
    WAITV(ta[7]);
    ISSUE_B(4, 8);
    {
      LAS unsigned char* kimg = L + L_KIMG + w * 8192;
#pragma unroll
      for (int r = 0; r < 8; ++r) {
        const int key = 4 * lg + (r & 3) + 16 * (r >> 2);
        *(LAS v4u*)(kimg + key * 256 + ((li ^ (key & 15)) << 4)) = ta[r];
      }
#pragma unroll
      for (int t = 0; t < 8; ++t) ta[t] = *(const LAS v4u*)(kimg + (li + 16 * (t >> 2)) * 256 + (((4 * (t & 3) + lg) ^ li) << 4));
    }
    const unsigned tag16 = (tid & 1) ? (tag >> 16) : (tag & 0xffffu);
    unsigned b1w;
    {
      const __amdgpu_buffer_rsrc_t rb1 = RSRC(p.b1, hasM ? 3584 * 2 : 0);
      b1w = __builtin_amdgcn_raw_buffer_load_b32(rb1, (fb * 128 + (tid & 63) * 2) * 2, 0, 0);
    }
    const __amdgpu_buffer_rsrc_t rw2 = RSRC(p.w2, hasM ? 3584 * 1024 * 2 : 0);
#define ISSUE_W2(lo, hi) do { const int voff = lg * 8 * 2048 + li * 16, soff = (fb * 128 + (w & 3) * 32) * 2048 + sub * 512 + (w >> 2) * 256; \
      _Pragma("unroll") for (int r = lo; r < hi; ++r) tc[r] = __builtin_amdgcn_raw_buffer_load_b128(rw2, voff, soff + r * 2048, W_AUX); SB(); } while (0)
    WAITV(tb[QG_AT]);
    {
      v2u qg[8];
      const int qoff = (int)WS_QP + (ab * 4096 + kvh * 1024 + tid * 2) * 4;
#define ISSUE_QG() _Pragma("unroll") for (int kk = 0; kk < 8; ++kk) qg[kk] = __builtin_amdgcn_raw_buffer_load_b64(rws, qoff, kk * (8 * 4096 * 4), SC1)
#define QG_OK() ((bad2q(qg[0], tag16) | bad2q(qg[1], tag16) | bad2q(qg[2], tag16) | bad2q(qg[3], tag16) | bad2q(qg[4], tag16) | bad2q(qg[5], tag16) | bad2q(qg[6], tag16) | bad2q(qg[7], tag16)) == 0u)
      ISSUE_QG();
      SB();
      tile_prep(tb);
      SB();
      if (!__all(QG_OK())) {
        int spins = 0;
        do { __builtin_amdgcn_s_sleep(PSLEEP); CB(); ISSUE_QG(); } while (!__all(QG_OK()) && ++spins < SPIN_CAP);
      }
      const float q0 = ((untagq(qg[0].x) + untagq(qg[1].x)) + (untagq(qg[2].x) + untagq(qg[3].x))) + ((untagq(qg[4].x) + untagq(qg[5].x)) + (untagq(qg[6].x) + untagq(qg[7].x)));
      const float q1 = ((untagq(qg[0].y) + untagq(qg[1].y)) + (untagq(qg[2].y) + untagq(qg[3].y))) + ((untagq(qg[4].y) + untagq(qg[5].y)) + (untagq(qg[6].y) + untagq(qg[7].y)));
      *(LAS unsigned*)(L + L_QS + (tid >> 6) * 272 + (tid & 63) * 4) = pk_bf16(q0, q1);
    }
    ISSUE_W2(0, 4);
    __syncthreads();
    {
      f32x4 d0 = {0.f, 0.f, 0.f, 0.f}, d1 = {0.f, 0.f, 0.f, 0.f};
#pragma unroll
      for (int t = 0; t < 4; ++t) {
        const v4u qf = *(const LAS v4u*)(L + L_QS + (li & 7) * 272 + t * 64 + lg * 16);
        d0 = mfma32(ta[t], qf, d0);
        d1 = mfma32(ta[4 + t], qf, d1);
      }
      const bool up = li >= 8;
      const unsigned r01 = pk_bf16(up ? d1[0] : d0[0], up ? d1[1] : d0[1]), r23 = pk_bf16(up ? d1[2] : d0[2], up ? d1[3] : d0[3]);
      const unsigned s01 = pk_bf16(bf_lo(r01) * 0.08838834764831845f, bf_hi(r01) * 0.08838834764831845f);
      const unsigned s23 = pk_bf16(bf_lo(r23) * 0.08838834764831845f, bf_hi(r23) * 0.08838834764831845f);
      const float s0 = bf_lo(s01), s1 = bf_hi(s01), s2 = bf_lo(s23), s3 = bf_hi(s23);
      float m = vmax(vmax3(s0, s1, s2), s3);
      m = vmax(m, dpp<0x128>(m)); m = x16_max(m); m = x32_max(m);
      const float mb = m * 1.4426950408889634f;
      const float p0 = __builtin_amdgcn_exp2f(fmaf(s0, 1.4426950408889634f, -mb)), p1 = __builtin_amdgcn_exp2f(fmaf(s1, 1.4426950408889634f, -mb));
      const float p2 = __builtin_amdgcn_exp2f(fmaf(s2, 1.4426950408889634f, -mb)), p3 = __builtin_amdgcn_exp2f(fmaf(s3, 1.4426950408889634f, -mb));
      const unsigned o01 = pk_bf16(p0, p1), o23 = pk_bf16(p2, p3), n01 = dppu8(o01), n23 = dppu8(o23);
      const v4u pb = {up ? n01 : o01, up ? n23 : o23, up ? o01 : n01, up ? o23 : n23};
      prepped_mfma_store(tb, pb, red0 + w * (8 * 132), lane);
      float ls = (p0 + p1) + (p2 + p3);
      ls += dpp<0x128>(ls); ls = x16_sum(ls); ls = x32_sum(ls);
      if (lane < 8) { *(LAS float*)(L + L_MX + lane * 32 + w * 4) = mb; *(LAS float*)(L + L_LS + lane * 32 + w * 4) = ls; }
    }
    __syncthreads();
    if (tid < 256) {
      const int hd = tid >> 5, d4 = tid & 31;
      const f32x4 ma = *(const LAS f32x4*)(L + L_MX + hd * 32), mb = *(const LAS f32x4*)(L + L_MX + hd * 32 + 16);
      const LAS float* r = red0 + hd * 132 + d4 * 4;
      f32x4 part[8];
#pragma unroll
      for (int ww = 0; ww < 8; ++ww) part[ww] = *(const LAS f32x4*)(r + ww * (8 * 132));
      const float M = vmax(vmax3(vmax3(ma.x, ma.y, ma.z), ma.w, mb.x), vmax3(mb.y, mb.z, mb.w));
      float f[8];
#pragma unroll
      for (int q = 0; q < 4; ++q) { f[q] = __builtin_amdgcn_exp2f(ma[q] - M); f[4 + q] = __builtin_amdgcn_exp2f(mb[q] - M); }
      f32x4 s = part[0] * f[0];
#pragma unroll
      for (int ww = 1; ww < 8; ++ww) s += part[ww] * f[ww];
      const int rec = ((kvh * 8 + hd) * 8 + ab) * 4 + sub;
      __builtin_amdgcn_raw_buffer_store_b128(tag4(s, tag), rws, (int)WS_OP + rec * 512 + d4 * 16, 0, SC1);
      if (d4 == 0) {
        const f32x4 la = *(const LAS f32x4*)(L + L_LS + hd * 32), lb = *(const LAS f32x4*)(L + L_LS + hd * 32 + 16);
        const float l = ((f[0] * la.x + f[1] * la.y) + (f[2] * la.z + f[3] * la.w)) + ((f[4] * lb.x + f[5] * lb.y) + (f[6] * lb.z + f[7] * lb.w));
        __builtin_amdgcn_raw_buffer_store_b128(tag4(f32x4{M, l, M, l}, tag), rws, (int)WS_ML + rec * 16, 0, SC1);
      }
    }
    ISSUE_W2(4, 8);
    WAITV(tc[HG_AT]);
    if (hasM) {
      v2u hg[4];
      const int hoff = (int)WS_HP + ((tid >> 6) * 3584 + fb * 128 + (tid & 63) * 2) * 4;
#define ISSUE_HG() _Pragma("unroll") for (int kk = 0; kk < 4; ++kk) hg[kk] = __builtin_amdgcn_raw_buffer_load_b64(rws, hoff, kk * (8 * 3584 * 4), SC1)
#define HG_OK() ((bad2(hg[0], tag16) | bad2(hg[1], tag16) | bad2(hg[2], tag16) | bad2(hg[3], tag16)) == 0u)
      ISSUE_HG();
      SB();
      tile_prep(tc);
      SB();
      if (!__all(HG_OK())) {
        int spins = 0;
        do { __builtin_amdgcn_s_sleep(PSLEEP); CB(); ISSUE_HG(); } while (!__all(HG_OK()) && ++spins < SPIN_CAP);
      }
      float hv[2];
      hv[0] = (untag(hg[0].x) + untag(hg[1].x)) + (untag(hg[2].x) + untag(hg[3].x));
      hv[1] = (untag(hg[0].y) + untag(hg[1].y)) + (untag(hg[2].y) + untag(hg[3].y));
#pragma unroll
      for (int q = 0; q < 2; ++q) {
        const float v = rbf(rbf(hv[q]) + (q ? bf_hi(b1w) : bf_lo(b1w)));
        const float u = 0.7978845608028654f * (v + 0.044715f * v * v * v);
        const float e = __builtin_amdgcn_exp2f(u * 2.8853900817779268f);
        const float th = 1.f - 2.f * __builtin_amdgcn_rcpf(e + 1.f);
        hv[q] = 0.5f * v * (1.f + th);
      }
      *(LAS unsigned*)(L + L_XS + (tid >> 6) * 272 + (tid & 63) * 4) = pk_bf16(hv[0], hv[1]);
    }
    __syncthreads();
    {
      const v4u bf3 = *(const LAS v4u*)(L + L_XS + (li & 7) * 272 + (w & 3) * 64 + lg * 16);
      prepped_mfma_store(tc, bf3, red1 + w * (8 * 132), lane);
    }
    __syncthreads();
    if (hasM) {
      const int b = tid >> 6, n4 = tid & 63;
      const LAS float* r = red1 + (n4 >> 5) * (4 * 8 * 132) + b * 132 + (n4 & 31) * 4;
      f32x4 s = *(const LAS f32x4*)r;
#pragma unroll
      for (int ww = 1; ww < 4; ++ww) s += *(const LAS f32x4*)(r + ww * (8 * 132));
      __builtin_amdgcn_raw_buffer_store_b128(tag4(s, tag), rws, (int)WS_YP + ((fb * 8 + b) * 1024 + sub * 256 + n4 * 4) * 4, 0, SC1);
    }
  }

  {
    const int part = tid >> 3, q4 = tid & 7;
    const bool act = part < 60;
    const int off = (part < 32 ? (int)WS_SP + part * (8 * 1024 * 4) : (int)WS_YP + (part - 32) * (8 * 1024 * 4)) + (xcd * 1024 + i * 32 + q4 * 4) * 4;
    const int offc = act ? off : (int)WS_END;
    u16 xw = 0, b2w = 0;
    if (tid < 32) { xw = p.x[xcd * 1024 + i * 32 + tid]; b2w = p.b2[i * 32 + tid]; }
    v4u f = __builtin_amdgcn_raw_buffer_load_b128(rws, offc, 0, SC1);
    int spins = 0;
    while (!__all((bad4(f, tag) == 0u) | !act) && ++spins < SPIN_CAP) { CB(); f = __builtin_amdgcn_raw_buffer_load_b128(rws, offc, 0, SC1); }
    f32x4 v = {untag(f.x), untag(f.y), untag(f.z), untag(f.w)};
#pragma unroll
    for (int q = 0; q < 4; ++q) { float a = v[q]; a += dpp<0x128>(a); a = x16_sum(a); v[q] = x32_sum(a); }
    if (lane < 8) *(LAS f32x4*)(L + L_FIN + w * 128 + lane * 16) = v;
    __syncthreads();
    if (tid < 32) {
      const LAS float* r = (const LAS float*)(L + L_FIN) + tid;
      const float s = (r[0] + r[32]) + (r[64] + r[96]), y = (r[128] + r[160]) + (r[192] + r[224]);
      const float yy = rbf(rbf(y) + bf_lo(b2w));
      const float e = __builtin_amdgcn_exp2f(-yy * 1.4426950408889634f);
      const float gt = rbf(yy * __builtin_amdgcn_rcpf(1.f + e));
      const float o = bf_lo(xw) + rbf(rbf(s) * gt);
      p.out[xcd * 1024 + i * 32 + tid] = (u16)pk_bf16(o, o);
      if (g == 0 && tid == 0) __hip_atomic_store((unsigned*)(p.ws + WS_EPOCH), tag, __ATOMIC_RELAXED, __HIP_MEMORY_SCOPE_AGENT);
    }
  }
}

extern "C" void kernel_launch(void* const* d_in, const int* in_sizes, int n_in, void* d_out, int out_size, void* d_ws, size_t ws_size,
                              hipStream_t stream) {
  Params p;
  p.x = (const u16*)d_in[0]; p.kc = (const u16*)d_in[1]; p.vc = (const u16*)d_in[2]; p.wq = (const u16*)d_in[3]; p.wo = (const u16*)d_in[4];
  p.w1 = (const u16*)d_in[6]; p.b1 = (const u16*)d_in[7]; p.w2 = (const u16*)d_in[8]; p.b2 = (const u16*)d_in[9];
  p.out = (u16*)d_out; p.ws = (unsigned char*)d_ws;
  constexpr int LDS = L_BYTES > 84 * 1024 ? L_BYTES : 84 * 1024;
  (void)hipFuncSetAttribute((const void*)fused_kernel, hipFuncAttributeMaxDynamicSharedMemorySize, LDS);
  hipLaunchKernelGGL(fused_kernel, dim3(256), dim3(512), LDS, stream, p);
}
```
